```python
import torch

def _softmax(x, axis=-1):
    return torch.softmax(x, dim=axis)

def _gelu(x):
    return torch.nn.functional.gelu(x, approximate='tanh')

def _silu(x):
    return torch.nn.functional.silu(x)

def _sigmoid(x):
    return torch.sigmoid(x)

def _layernorm(x, axis=-1, eps=1e-05):
    m = x.mean(dim=axis, keepdim=True)
    v = x.var(dim=axis, keepdim=True, unbiased=False)
    return (x - m) / torch.sqrt(v + eps)

def _scatter_add(tgt, idx, src, axis=0):
    return tgt.clone().index_add_(axis, idx, src)

def _segment_sum(src, seg, n):
    out = torch.zeros((n,) + src.shape[1:], dtype=src.dtype)
    return out.index_add_(0, seg, src)

def _pad(x, pw):
    flat = tuple((v for pair in reversed(pw) for v in pair))
    return torch.nn.functional.pad(x, flat)
kernel_name = 'adaLN_cond__linear_kernel_hedgehog__geglu_ffn__prompt'

def setup_inputs():

    _w = lambda a: (lambda t: t.to(torch.bfloat16) if t.is_floating_point() else t)(torch.as_tensor(a))

    def _A_setup():
        B, S, D, H = (1, 2048, 448, 16)
        Dh = D // H
        return {'x': _w((lambda *s: torch.randn(*s, dtype=torch.bfloat16) * 0.05)(B, S, D)), 'wqkv': _w((lambda *s: torch.randn(*s, dtype=torch.bfloat16) * 0.4)(D, 3 * D)), 'w_phi': _w((lambda *s: torch.randn(*s, dtype=torch.bfloat16) * 0.4)(H, Dh, Dh) * Dh ** (-0.5)), 'wo': _w((lambda *s: torch.randn(*s, dtype=torch.bfloat16) * 0.05)(D, D))}
    _A_in = _A_setup()

    def _B_setup():
        B, S, D, F = (1, 2048, 448, 1792)
        return {'x': _w((lambda *s: torch.randn(*s, dtype=torch.bfloat16) * 0.05)(B, S, D)), 'w_gate': _w((lambda *s: torch.randn(*s, dtype=torch.bfloat16) * 0.05)(D, F)), 'w_up': _w((lambda *s: torch.randn(*s, dtype=torch.bfloat16) * 0.05)(D, F)), 'w_down': _w((lambda *s: torch.randn(*s, dtype=torch.bfloat16) * 0.05)(F, D))}
    _B_in = _B_setup()
    return {'A_x': _A_in['x'], 'A_wqkv': _A_in['wqkv'], 'A_w_phi': _A_in['w_phi'], 'A_wo': _A_in['wo'], 'B_x': _B_in['x'], 'B_w_gate': _B_in['w_gate'], 'B_w_up': _B_in['w_up'], 'B_w_down': _B_in['w_down'], 'c': _w((lambda *s: torch.randn(*s, dtype=torch.bfloat16) * 0.1)(1, 448) * 8.0), 'w_mod': _w((lambda *s: torch.randn(*s, dtype=torch.bfloat16) * 0.1)(448, 6 * 448) * 448 ** (-0.5) * 8.0)}

def reference(A_x, A_wqkv, A_w_phi, A_wo, B_x, B_w_gate, B_w_up, B_w_down, c, w_mod):

    def _A_ref(x, wqkv, w_phi, wo):
        B, S, D = x.shape
        H = 16
        Dh = D // H
        qkv = (lambda x, *d: x.permute(*d))((x @ wqkv).reshape(B, S, 3, H, Dh), 2, 0, 3, 1, 4)
        q, k, v = (qkv[0], qkv[1], qkv[2])
        pq = torch.einsum('bhsd,hdf->bhsf', q, w_phi)
        pk = torch.einsum('bhsd,hdf->bhsf', k, w_phi)
        phi_q = torch.concatenate([_softmax(pq, axis=-1), _softmax(-pq, axis=-1)], axis=-1)
        phi_k = torch.concatenate([_softmax(pk, axis=-1), _softmax(-pk, axis=-1)], axis=-1)
        kv = torch.einsum('bhsf,bhsd->bhfd', phi_k, v)
        z = torch.einsum('bhsf->bhf', phi_k)
        num = torch.einsum('bhqf,bhfd->bhqd', phi_q, kv)
        den = torch.einsum('bhqf,bhf->bhq', phi_q, z)[..., None] + 1e-06
        o = num / den
        return (lambda x, *d: x.permute(*d))(o, 0, 2, 1, 3).reshape(B, S, D) @ wo

    def _B_ref(x, w_gate, w_up, w_down):
        return _gelu(x @ w_gate) * (x @ w_up) @ w_down
    x = A_x
    mod = (_silu(c) @ w_mod).reshape(c.shape[0], 6, 1, -1)
    sa, ca, ga, sb, cb, gb = (mod[:, 0].clone(), mod[:, 1].clone(), mod[:, 2].clone(), mod[:, 3].clone(), mod[:, 4].clone(), mod[:, 5].clone())
    h = _layernorm(x, axis=-1) * (1 + ca) + sa
    x = x + ga * _A_ref(h, A_wqkv, A_w_phi, A_wo)
    h = _layernorm(x, axis=-1) * (1 + cb) + sb
    x = x + gb * _B_ref(h, B_w_gate, B_w_up, B_w_down)
    return x
```

```cpp
#include <hip/hip_runtime.h>
#include <hip/hip_bf16.h>
typedef unsigned short u16;
__device__ __forceinline__ float b2f(u16 b){ return __uint_as_float(((unsigned)b)<<16); }
__device__ __forceinline__ u16 f2b(float f){ unsigned u=__float_as_uint(f); u += 0x7fffu + ((u>>16)&1u); return (u16)(u>>16); }
constexpr int S=2048,D=448,H=16,DH=28,F=1792;

__global__ void k_mod(const u16* c,const u16* wmod,float* mod){
  int n=blockIdx.x*blockDim.x+threadIdx.x; if(n>=6*D) return;
  float acc=0; for(int k=0;k<D;k++){ float x=b2f(c[k]); float s=x/(1.f+__expf(-x)); s=b2f(f2b(s)); acc+=s*b2f(wmod[k*6*D+n]); }
  mod[n]=b2f(f2b(acc));
}
__global__ void k_ln(const u16* xb,const float* xf,const float* shift,const float* scale,float* h){
  int r=blockIdx.x; __shared__ float red[64]; int t=threadIdx.x;
  float v[7]; float s=0; for(int i=0;i<7;i++){ int k=t+64*i; v[i]= xb? b2f(xb[r*D+k]) : xf[r*D+k]; s+=v[i]; }
  for(int o=32;o;o>>=1) s+=__shfl_xor(s,o); float m=s/D; float q=0; for(int i=0;i<7;i++){ float d=v[i]-m; q+=d*d; }
  for(int o=32;o;o>>=1) q+=__shfl_xor(q,o); float rs=1.f/sqrtf(q/D+1e-5f);
  for(int i=0;i<7;i++){ int k=t+64*i; h[r*D+k]=b2f(f2b((v[i]-m)*rs*(1.f+scale[k])+shift[k])); }
}
__global__ void k_gemm(const float* A,const u16* W,float* C,int M,int N,int K){
  int n=blockIdx.x*blockDim.x+threadIdx.x; int m=blockIdx.y; if(n>=N) return;
  float acc=0; for(int k=0;k<K;k++) acc+=A[m*K+k]*b2f(W[k*N+n]); C[m*N+n]=b2f(f2b(acc));
}
__global__ void k_phi(const float* qkv,const u16* wphi,float* phiq,float* phik){
  int idx=blockIdx.x*blockDim.x+threadIdx.x;
  if(idx>=S*H*2) return; int which=idx&1; int h=(idx>>1)%H; int s=idx/(2*H);
  const float* q=qkv+s*3*D+which*D+h*DH; float* out=(which?phik:phiq)+(s*H+h)*56;
  float mx=-1e30f,mn=1e30f;
  for(int f=0;f<DH;f++){ float a=0; for(int d=0;d<DH;d++) a+=q[d]*b2f(wphi[(h*DH+d)*DH+f]); out[f]=a; mx=fmaxf(mx,a); mn=fminf(mn,a);}
  float s1=0,s2=0; for(int f=0;f<DH;f++){ float a=out[f]; float e1=__expf(a-mx), e2=__expf(mn-a); out[f]=e1; out[28+f]=e2; s1+=e1; s2+=e2; }
  for(int f=0;f<DH;f++){ out[f]/=s1; out[28+f]/=s2; }
}
__global__ void k_kv(const float* phik,const float* qkv,float* kv){
  int idx=blockIdx.x*blockDim.x+threadIdx.x; if(idx>=H*56*29) return; int d=idx%29; int f=(idx/29)%56; int h=idx/(29*56);
  float a=0; for(int s=0;s<S;s++){ float p=phik[(s*H+h)*56+f]; a+= p*(d<28? qkv[s*3*D+2*D+h*DH+d]:1.f);} kv[idx]=a;
}
__global__ void k_o(const float* phiq,const float* kv,float* o){
  int idx=blockIdx.x*blockDim.x+threadIdx.x; if(idx>=S*D) return; int d=idx%DH; int h=(idx/DH)%H; int s=idx/D;
  float num=0,den=0; for(int f=0;f<56;f++){ float p=phiq[(s*H+h)*56+f]; num+=p*kv[(h*56+f)*29+d]; den+=p*kv[(h*56+f)*29+28]; }
  o[idx]=b2f(f2b(num/(den+1e-6f)));
}
__global__ void k_res1(const u16* x,const float* a,const float* g,float* x1){ int i=blockIdx.x*blockDim.x+threadIdx.x; if(i>=S*D) return; x1[i]=b2f(f2b(b2f(x[i])+b2f(f2b(g[i%D]*a[i])))); }
__global__ void k_act(const float* g,const float* u,float* a){ int i=blockIdx.x*blockDim.x+threadIdx.x; if(i>=S*F) return; float x=g[i]; float t=tanhf(0.7978845608f*(x+0.044715f*x*x*x)); a[i]=b2f(f2b(b2f(f2b(0.5f*x*(1.f+t)))*u[i])); }
__global__ void k_res2(const float* x1,const float* a,const float* g,u16* out){ int i=blockIdx.x*blockDim.x+threadIdx.x; if(i>=S*D) return; out[i]=f2b(x1[i]+b2f(f2b(g[i%D]*a[i]))); }

extern "C" void kernel_launch(void* const* d_in, const int* in_sizes, int n_in, void* d_out, int out_size, void* d_ws, size_t ws_size, hipStream_t stream) {
  const u16* x=(const u16*)d_in[0]; const u16* wqkv=(const u16*)d_in[1]; const u16* wphi=(const u16*)d_in[2]; const u16* wo=(const u16*)d_in[3];
  const u16* wg=(const u16*)d_in[5]; const u16* wu=(const u16*)d_in[6]; const u16* wd=(const u16*)d_in[7]; const u16* c=(const u16*)d_in[8]; const u16* wmod=(const u16*)d_in[9];
  float* ws=(float*)d_ws; float* mod=ws; ws+=4096; float* h=ws; ws+=S*D; float* qkv=ws; ws+=S*3*D; float* phiq=ws; ws+=S*H*56; float* phik=ws; ws+=S*H*56;
  float* kv=ws; ws+=32768; float* o=ws; ws+=S*D; float* ao=ws; ws+=S*D; float* x1=ws; ws+=S*D; float* g=ws; ws+=S*F; float* u=ws; ws+=S*F; float* a=ws; ws+=S*F; float* dn=ws; ws+=S*D;
  k_mod<<<(6*D+63)/64,64,0,stream>>>(c,wmod,mod);
  k_ln<<<S,64,0,stream>>>(x,nullptr,mod,mod+D,h);
  k_gemm<<<dim3((3*D+63)/64,S),64,0,stream>>>(h,wqkv,qkv,S,3*D,D);
  k_phi<<<(S*H*2+63)/64,64,0,stream>>>(qkv,wphi,phiq,phik);
  k_kv<<<(H*56*29+63)/64,64,0,stream>>>(phik,qkv,kv);
  k_o<<<(S*D+255)/256,256,0,stream>>>(phiq,kv,o);
  k_gemm<<<dim3((D+63)/64,S),64,0,stream>>>(o,wo,ao,S,D,D);
  k_res1<<<(S*D+255)/256,256,0,stream>>>(x,ao,mod+2*D,x1);
  k_ln<<<S,64,0,stream>>>(nullptr,x1,mod+3*D,mod+4*D,h);
  k_gemm<<<dim3((F+63)/64,S),64,0,stream>>>(h,wg,g,S,F,D);
  k_gemm<<<dim3((F+63)/64,S),64,0,stream>>>(h,wu,u,S,F,D);
  k_act<<<(S*F+255)/256,256,0,stream>>>(g,u,a);
  k_gemm<<<dim3((D+63)/64,S),64,0,stream>>>(a,wd,dn,S,D,F);
  k_res2<<<(S*D+255)/256,256,0,stream>>>(x1,dn,mod+5*D,(u16*)d_out);
}
```

```cpp
#include <hip/hip_runtime.h>

typedef unsigned u32;
typedef unsigned long long u64;
typedef u32 v4u __attribute__((ext_vector_type(4)));
typedef u32 v2u __attribute__((ext_vector_type(2)));
typedef float f16v __attribute__((ext_vector_type(16)));
typedef float f4v __attribute__((ext_vector_type(4)));
typedef float f2v __attribute__((ext_vector_type(2)));
typedef __bf16 bf8 __attribute__((ext_vector_type(8)));
typedef __bf16 bf2 __attribute__((ext_vector_type(2)));
typedef short s4v __attribute__((ext_vector_type(4)));
typedef __amdgpu_buffer_rsrc_t rsrc_t;
#define DI __device__ __forceinline__
#define LAS __attribute__((address_space(3)))

namespace {

constexpr int S_ = 2048, D_ = 448, F_ = 1792;
constexpr u32 OOB = 0x7ff00000u;
constexpr int SC1 = 16;
constexpr u32 SPIN_CAP = 1u << 16;

constexpr u32 WS_EPOCH = 0;
constexpr u32 WS_FO = 4096;
constexpr u32 WS_FY = WS_FO + 4096;
constexpr u32 WS_FW = WS_FY + 4096;
constexpr u32 WS_FWO = WS_FW + 4096;
constexpr u32 WS_FWD = WS_FWO + 1024;
constexpr u32 WS_FA = WS_FWD + 3072;
constexpr u32 WS_MG0 = WS_FA + 32768;
constexpr u32 WS_MG1 = WS_MG0 + 16384;
constexpr u32 WS_KV2 = WS_MG1 + 114688;
constexpr u32 WS_ST = WS_KV2 + 131072;
constexpr u32 WS_UG = WS_ST + 262144;
constexpr u32 WS_WOT = WS_UG + 458752;
constexpr u32 WS_KV1 = WS_WOT + 458752;
constexpr u32 WS_O = WS_KV1 + 4194304;
constexpr u32 WS_Y = WS_O + 2097152;
constexpr u32 WS_WGT = WS_Y + 1835008;
constexpr u32 WS_WDT = WS_WGT + 3211264;
constexpr u32 WS_ACT = WS_WDT + 1605632;
constexpr u32 WS_END = WS_ACT + 7340032;
constexpr u32 WS_STAMP = WS_END;

constexpr u32 L_WP = 0;
constexpr u32 L_EXT = 86016;
constexpr u32 L_XR = 89088;
constexpr u32 L_PHI = 154624;
constexpr u32 L_SB = 156672;
constexpr u32 L_GS = 160768;
constexpr u32 L_DUMMY = 162560;
constexpr u32 L_VEC = 162816;
constexpr u32 L_PREP = 57344;
constexpr u32 L_IMG = 0;
constexpr u32 L_X1 = 154624;
constexpr u32 L_Y = 0, L_YEXT = 114688, L_SIG = 118784;
constexpr u32 L_TOTAL = 163840;

DI f16v mfma32(v4u a, v4u b, f16v c) {
  return __builtin_amdgcn_mfma_f32_32x32x16_bf16(__builtin_bit_cast(bf8, a), __builtin_bit_cast(bf8, b), c, 0, 0, 0);
}
DI f4v mfma16(v4u a, v4u b, f4v c) {
  return __builtin_amdgcn_mfma_f32_16x16x32_bf16(__builtin_bit_cast(bf8, a), __builtin_bit_cast(bf8, b), c, 0, 0, 0);
}
DI u32 pk(float a, float b) { return __builtin_bit_cast(u32, __builtin_convertvector(f2v{a, b}, bf2)); }
DI float lo2f(u32 w) { return __builtin_bit_cast(float, w << 16); }
DI float hi2f(u32 w) { return __builtin_bit_cast(float, w & 0xffff0000u); }
DI float rbf(float a) { return lo2f(pk(a, a)); }
DI u32 f2u(float a) { return __builtin_bit_cast(u32, a); }
DI float u2f(u32 a) { return __builtin_bit_cast(float, a); }
DI float dot2(u32 a, u32 b, float c) {
  return __builtin_amdgcn_fdot2_f32_bf16(__builtin_bit_cast(bf2, a), __builtin_bit_cast(bf2, b), c, false);
}
DI u32 hilo(float a) { const float h = rbf(a); return pk(h, a - h); }
DI float sum32(float a) { auto r = __builtin_amdgcn_permlane32_swap(f2u(a), f2u(a), false, false); return u2f(r[0]) + u2f(r[1]); }
DI float sum16(float a) { auto r = __builtin_amdgcn_permlane16_swap(f2u(a), f2u(a), false, false); return u2f(r[0]) + u2f(r[1]); }
DI float max32(float a) { auto r = __builtin_amdgcn_permlane32_swap(f2u(a), f2u(a), false, false); return fmaxf(u2f(r[0]), u2f(r[1])); }
DI float min32(float a) { auto r = __builtin_amdgcn_permlane32_swap(f2u(a), f2u(a), false, false); return fminf(u2f(r[0]), u2f(r[1])); }
DI rsrc_t mk(const void* p, u32 bytes) { return __builtin_amdgcn_make_buffer_rsrc((void*)p, 0, bytes, 0x00020000); }
DI int fidx(int row) { return row < 26 ? row : ((row == 28 || row == 29) ? row - 2 : -1); }

DI void dma(u32 lds, u32 voff, rsrc_t rs, u32 soff) {
  asm volatile("s_mov_b32 m0, %0\n\ts_nop 0\n\tbuffer_load_dwordx4 %1, %2, %3 offen lds" ::"s"(lds), "v"(voff), "s"(rs), "s"(soff) : "memory");
}
DI void dma_sc1(u32 lds, u32 voff, rsrc_t rs, u32 soff) {
  asm volatile("s_mov_b32 m0, %0\n\ts_nop 0\n\tbuffer_load_dwordx4 %1, %2, %3 offen sc1 lds" ::"s"(lds), "v"(voff), "s"(rs), "s"(soff) : "memory");
}
template <int N> DI void vmwait() { asm volatile("s_waitcnt vmcnt(%0)" ::"n"(N) : "memory"); }
DI void lgwait() { asm volatile("s_waitcnt lgkmcnt(0)" ::: "memory"); }
DI void bar() { asm volatile("s_waitcnt lgkmcnt(0)\n\ts_barrier" ::: "memory"); }
DI v4u gld(rsrc_t rs, u32 voff, u32 soff) { return __builtin_amdgcn_raw_buffer_load_b128(rs, voff, soff, SC1); }
DI u32 gld1(rsrc_t rs, u32 voff, u32 soff) { return __builtin_amdgcn_raw_buffer_load_b32(rs, voff, soff, SC1); }
DI void gst(v4u v, rsrc_t rs, u32 voff, u32 soff) { __builtin_amdgcn_raw_buffer_store_b128(v, rs, voff, soff, SC1); }
DI void gst2(v2u v, rsrc_t rs, u32 voff, u32 soff) { __builtin_amdgcn_raw_buffer_store_b64(v, rs, voff, soff, SC1); }
DI void gst1(u32 v, rsrc_t rs, u32 voff, u32 soff) { __builtin_amdgcn_raw_buffer_store_b32(v, rs, voff, soff, SC1); }
DI void order() { asm volatile("" ::: "memory"); }
DI void poll_flags(rsrc_t rs, u32 soff, int lane, int n, u32 tag) {
  const u32 vo = (lane < n ? lane : 0) * 4;
  for (u32 sp = 0; sp < SPIN_CAP; ++sp) {
    order();
    const u32 v = gld1(rs, vo, soff);
    if (__all(v == tag)) break;
  }
  order();
}

#define STAMP(i)
#define STAMP_FLUSH()

DI void feature_map(const f16v& acc, float rl, v4u (&ph)[4]) {
  float mx = acc[0], mn = acc[0];
#pragma unroll
  for (int i = 1; i < 14; ++i) { mx = fmaxf(mx, acc[i]); mn = fminf(mn, acc[i]); }
  mx = max32(mx); mn = min32(mn);
  const float cp = -mx * rl, cn = mn * rl;
  float ep[14], en[14], sp = 0.f, sn = 0.f;
#pragma unroll
  for (int i = 0; i < 14; ++i) {
    ep[i] = __builtin_amdgcn_exp2f(fmaf(acc[i], rl, cp));
    en[i] = __builtin_amdgcn_exp2f(fmaf(acc[i], -rl, cn));
    sp += ep[i]; sn += en[i];
  }
  const float ip = __builtin_amdgcn_rcpf(sum32(sp)), in = __builtin_amdgcn_rcpf(sum32(sn));
#pragma unroll
  for (int i = 0; i < 7; ++i) {
    ph[i >> 2][i & 3] = pk(ep[2 * i] * ip, ep[2 * i + 1] * ip);
    ph[2 + (i >> 2)][i & 3] = pk(en[2 * i] * in, en[2 * i + 1] * in);
  }
  ph[1][3] = 0u; ph[3][3] = 0u;
}

}

typedef u32 v16u __attribute__((ext_vector_type(16)));
typedef u32 v8u __attribute__((ext_vector_type(8)));
DI void* mkptr(u32 lo, u32 hi) { return (void*)(((u64)hi << 32) | lo); }

__global__ void __launch_bounds__(512, 2)
fused_block(const void*, const void*, const void*, const void*, const void*, const void*, const void*, const void*, const void*, const void*,
            void*, void*) {
  v16u ka;
  v8u kb_;
  asm volatile("s_load_dwordx16 %0, %2, 0x0\n\ts_load_dwordx8 %1, %2, 0x40" : "=&s"(ka), "=&s"(kb_) : "s"(__builtin_amdgcn_kernarg_segment_ptr()) : "memory");
  __shared__ __attribute__((aligned(16))) char L[L_TOTAL];
#define LD128(o) (*(LAS v4u*)((LAS char*)L + (o)))
#define LD64(o) (*(LAS v2u*)((LAS char*)L + (o)))
#define LD32(o) (*(LAS u32*)((LAS char*)L + (o)))
#define LD16(o) (*(LAS unsigned short*)((LAS char*)L + (o)))
#define LDF4(o) (*(LAS f4v*)((LAS char*)L + (o)))
#define LDTR(o) __builtin_bit_cast(v2u, __builtin_amdgcn_ds_read_tr16_b64_v4i16((LAS s4v*)((LAS char*)L + (o))))
  const u32 LB = (u32)(size_t)(LAS char*)L;
  const int b = blockIdx.x, lane = threadIdx.x & 63, w = __builtin_amdgcn_readfirstlane(threadIdx.x >> 6);
  const int xg = b & 7, jb = b >> 3;
  const int mt = (xg >> 1) * 4 + (jb >> 3), head = (xg & 1) * 8 + (jb & 7);
  const bool ffn = jb >= 4;
  const int jj = ffn ? jb - 4 : 0, jq = jj / 7, cs = jj - 7 * jq;
  const int tt64 = xg * 4 + jq, mt3 = (xg >> 1) * 4 + jq, fs = (xg & 1) * 7 + cs;
  const int r32 = lane & 31, h = lane >> 5, wi = w & 3;
  const bool gbig = b < 28;
  const int ge = gbig ? 0 : (b < 252 ? b - 28 : 0);
  const int gk = gbig ? (b >= 14) : (ge * 2341) >> 16, gblk = gbig ? b - 14 * gk : 14 + ge - 28 * gk;
  u32 grow = w < 7 ? (gbig ? gk * 224 + w * 32 : gk * 56 + w * 8) : 0;
  u32 gso = grow * 5376 + gblk * 128;
  u32 vw = ((lane >> 3) * 5376 + (lane & 7) * 16) | (w < 7 ? 0 : OOB), vc = ((lane >> 3) * 2) | (w < 7 ? 0 : OOB);
  u32 vw2 = vw | (gbig ? 0 : OOB), vc2 = vc | (gbig ? 0 : OOB);
  asm volatile("s_waitcnt lgkmcnt(0)" : "+s"(ka), "+s"(kb_), "+s"(grow), "+s"(gso), "+v"(vw), "+v"(vc), "+v"(vw2), "+v"(vc2)::"memory");
  const void *px = mkptr(ka[0], ka[1]), *pwqkv = mkptr(ka[2], ka[3]), *pwphi = mkptr(ka[4], ka[5]), *pwo = mkptr(ka[6], ka[7]),
             *pwg = mkptr(ka[10], ka[11]), *pwu = mkptr(ka[12], ka[13]), *pwd = mkptr(ka[14], ka[15]), *pc = mkptr(kb_[0], kb_[1]),
             *pwmod = mkptr(kb_[2], kb_[3]);
  void *pout = mkptr(kb_[4], kb_[5]), *ws = mkptr(kb_[6], kb_[7]);
  v4u gw[4];
  u32 gc[4];
  {
    const rsrc_t rM = mk(pwmod, 448 * 2688 * 2), rC = mk(pc, 896);
#pragma unroll
    for (int i = 0; i < 4; ++i) {
      asm volatile("buffer_load_dwordx4 %0, %1, %2, %3 offen" : "=&v"(gw[i]) : "v"(i ? vw2 + i * 43008 : vw), "s"(rM), "s"(gso) : "memory");
      asm volatile("buffer_load_ushort %0, %1, %2, %3 offen offset:%4" : "=&v"(gc[i]) : "v"(i ? vc2 : vc), "s"(rC), "s"(grow * 2), "n"(i * 16) : "memory");
    }
  }
  STAMP(0);
  u32 epoch;
  asm volatile("s_load_dword %0, %1, 0x0" : "=s"(epoch) : "s"(ws) : "memory");
  const rsrc_t rW = mk(ws, WS_END);
  if (w == 7) {
    const rsrc_t rP = mk(pwphi, 16 * 28 * 28 * 2);
    dma(LB + L_PHI, lane * 16, rP, head * 1568);
    dma(LB + L_PHI + 1024, lane * 16, rP, head * 1568 + 1024);
  }
  const bool pwo_ = b >= 28 && b < 56, pgu = b >= 60;
  const int pu = pgu ? b - 60 : (pwo_ ? b - 28 : 0), pks = pu % 7, pfb = pu / 7;
  const rsrc_t rX = mk(px, S_ * D_ * 2), rQ = mk(pwqkv, 448 * 1344 * 2);
  const u32 vraw = (lane >> 2) * 2688 + (((lane & 3) ^ ((lane >> 4) & 3)) * 16);
  const u32 vx = (lane >> 3) * 896 + (((lane & 7) ^ (lane >> 4)) * 16);
  if (w < 4) {
    {
      const rsrc_t rP = pgu ? mk(w < 2 ? pwg : pwu, 448 * 1792 * 2) : mk(pwo, 448 * 448 * 2);
      const u32 rs_ = pgu ? 3584 : 896;
      const u32 vp = (lane >> 3) * rs_ + (lane & 7) * 16;
      const u32 row0 = pgu ? pks * 64 + (w & 1) * 32 : pfb * 112 + w * 32, col = pgu ? pfb * 128 : pks * 128;
#pragma unroll
      for (int i = 0; i < 4; ++i) {
        const bool on = (pgu || pwo_) && row0 + 8 * i < 448;
        dma(LB + L_PREP + (w * 4 + i) * 1024, vp | (on ? 0 : OOB), rP, on ? (row0 + 8 * i) * rs_ + col : 0);
      }
    }
#pragma unroll
    for (int c = 0; c < 4; ++c)
#pragma unroll
      for (int i = 0; i < 4; ++i)
        dma(LB + L_XR + w * 16384 + c * 4096 + i * 1024, vx ^ ((i & 1) * 64), rX, (mt * 128 + w * 32 + i * 8) * 896 + c * 128);
  } else {
#pragma unroll
    for (int q = 0; q < 4; ++q) {
      const int rho = (q + 3) & 3, kb = wi + 4 * rho;
      if (rho < 3 || wi < 2) {
#pragma unroll
        for (int p = 0; p < 2; ++p)
#pragma unroll
          for (int u = 0; u < 2; ++u)
            dma(LB + L_WP + p * 28672 + kb * 2048 + u * 1024, vraw, rQ, (kb * 32 + u * 16) * 2688 + (p + 1) * 896 + head * 56);
      }
    }
  }
  asm volatile("s_waitcnt vmcnt(20)\n\ts_cmp_lt_u32 %8, 4\n\ts_cbranch_scc1 1f\n\ts_waitcnt vmcnt(16)\n\ts_cmp_lt_u32 %8, 6\n\ts_cbranch_scc1 1f\n\ts_waitcnt vmcnt(12)\n1:"
               : "+v"(gw[0]), "+v"(gw[1]), "+v"(gw[2]), "+v"(gw[3]), "+v"(gc[0]), "+v"(gc[1]), "+v"(gc[2]), "+v"(gc[3]) : "s"(w) : "memory", "scc");
  STAMP(1);
  if (w < 7) {
    float a[8];
#pragma unroll
    for (int n = 0; n < 8; ++n) a[n] = 0.f;
#pragma unroll
    for (int i = 0; i < 4; ++i) {
      if (i == 0 || gbig) {
        const float cv = lo2f(gc[i]);
        const float sc = rbf(cv * __builtin_amdgcn_rcpf(1.f + __builtin_amdgcn_exp2f(-1.44269504f * cv)));
#pragma unroll
        for (int n = 0; n < 4; ++n) { a[2 * n] = fmaf(sc, lo2f(gw[i][n]), a[2 * n]); a[2 * n + 1] = fmaf(sc, hi2f(gw[i][n]), a[2 * n + 1]); }
      }
    }
    float e[4], f[2];
#pragma unroll
    for (int n = 0; n < 4; ++n) { auto r = __builtin_amdgcn_permlane32_swap(f2u(a[n]), f2u(a[n + 4]), false, false); e[n] = u2f(r[0]) + u2f(r[1]); }
#pragma unroll
    for (int n = 0; n < 2; ++n) { auto r = __builtin_amdgcn_permlane16_swap(f2u(e[n]), f2u(e[n + 2]), false, false); f[n] = u2f(r[0]) + u2f(r[1]); }
    const float t0 = f[0] + u2f(__builtin_amdgcn_update_dpp(0u, f2u(f[0]), 0x128, 0xf, 0xf, true));
    const float t1 = f[1] + u2f(__builtin_amdgcn_update_dpp(0u, f2u(f[1]), 0x128, 0xf, 0xf, true));
    LD32(L_GS + w * 256 + ((lane & 7) * 8 + (lane >> 3)) * 4) = f2u((lane & 8) ? t1 : t0);
  }
  if (w < 4) vmwait<16>();
  asm volatile("s_waitcnt lgkmcnt(0)\n\ts_barrier" : "+s"(epoch)::"memory");
  const u32 tag = epoch * 1664525u + 1013904223u;
  STAMP(2);

  v4u Wf[29];

  if (w < 4) {
    v4u pf[4];
    {
      const int g = lane >> 4, a_ = (lane >> 2) & 3, p = lane & 3;
#pragma unroll
      for (int e = 0; e < 4; ++e) {
        u32 o0, o1;
        if (pgu) {
          o0 = L_PREP + (g & 1) * 8192 + (16 * e + 8 * (g >> 1) + a_) * 128 + w * 32 + p * 8; o1 = o0 + 512;
        } else {
          const int d0 = 16 * (g >> 1) + 4 * (g & 1) + a_, f1 = fidx(d0 + 8);
          o0 = L_PREP + (w * 28 + d0) * 128 + p * 32 + e * 8;
          o1 = L_PREP + (w * 28 + (f1 < 0 ? 0 : f1)) * 128 + p * 32 + e * 8;
        }
        const v2u t0 = LDTR(o0), t1 = LDTR(o1);
        pf[e] = v4u{t0.x, t0.y, t1.x, t1.y};
      }
      const u32 so = pgu ? WS_WGT + ((pfb * 4 + w) * 28 + pks * 4) * 1024 : WS_WOT + (pks * 16 + pfb * 4 + w) * 4096;
      const u32 vo = (lane * 16) | ((pgu || pwo_) ? 0 : OOB);
#pragma unroll
      for (int e = 0; e < 4; ++e) gst(pf[e], rW, vo + e * 1024, so);
      order();
    }
    v4u xf[28];
    float sx = 0.f, sq = 0.f;
    {
      const u32 xo = L_XR + w * 16384 + r32 * 128, sw = (r32 >> 1) & 7;
#pragma unroll
      for (int c = 0; c < 7; ++c) {
        if (c < 4) vmwait<16>(); else if (c == 4) vmwait<8>(); else if (c == 5) vmwait<5>(); else vmwait<1>();
        if (c == 4) {
          const u32 so = pgu ? WS_FW + ((pfb * 4 + w) * 8 + pks) * 4 : WS_FWO + (pks * 16 + pfb * 4 + w) * 4;
          gst1(tag, rW, (lane == 0 && (pgu || pwo_)) ? 0 : OOB, so);
          order();
        }
#pragma unroll
        for (int t = 0; t < 4; ++t) xf[4 * c + t] = LD128(xo + (c & 3) * 4096 + (((2 * t + h) ^ sw) * 16));
        if (c < 3) {
          asm volatile("s_waitcnt lgkmcnt(0)" : "+v"(xf[4 * c]), "+v"(xf[4 * c + 1]), "+v"(xf[4 * c + 2]), "+v"(xf[4 * c + 3])::"memory");
#pragma unroll
          for (int i = 0; i < 4; ++i)
            dma(LB + L_XR + w * 16384 + c * 4096 + i * 1024, vx ^ ((i & 1) * 64), rX, (mt * 128 + w * 32 + i * 8) * 896 + (c + 4) * 128);
        }
#pragma unroll
        for (int t = 0; t < 4; ++t)
#pragma unroll
          for (int n = 0; n < 4; ++n) { sx = dot2(xf[4 * c + t][n], 0x3f803f80u, sx); sq = dot2(xf[4 * c + t][n], xf[4 * c + t][n], sq); }
      }
    }
    const float mean = sum32(sx) * (1.f / 448.f);
    const float var = fmaxf(sum32(sq) * (1.f / 448.f) - mean * mean, 0.f);
    const float sig = rbf(__builtin_sqrtf(rbf(rbf(var) + 1e-5f)));
    const float rs = 1.f / sig, rl = rs * 1.44269504f;
    const u32 e0 = pk(-mean, -mean), e1 = pk(sig, sig);
    const v4u xe = v4u{e0, e1, e0, e1};
    STAMP(3);
    f16v aK, aV;
#pragma unroll
    for (int i = 0; i < 16; ++i) { aK[i] = 0.f; aV[i] = 0.f; }
#pragma unroll
    for (int rho = 0; rho < 4; ++rho) {
      bar();
      if (rho == 0) STAMP(4);
#pragma unroll
      for (int k = 8 * rho; k < (rho < 3 ? 8 * rho + 8 : 28); ++k) {
        aK = mfma32(LD128(L_WP + k * 1024 + lane * 16), xf[k], aK);
        aV = mfma32(LD128(L_WP + 28672 + k * 1024 + lane * 16), xf[k], aV);
      }
    }
    aK = mfma32(LD128(L_EXT + lane * 16), xe, aK);
    aV = mfma32(LD128(L_EXT + 1024 + lane * 16), xe, aV);
    STAMP(5);
    {
      v4u ph[4];
      feature_map(aK, rl, ph);
      u32 vv[8];
#pragma unroll
      for (int i = 0; i < 7; ++i) vv[i] = pk(aV[2 * i] * rs, aV[2 * i + 1] * rs);
      vv[7] = 0x00003f80u;
      const u32 io = L_IMG + (w * 32 + r32) * 64, f = (((r32 >> 2) & 1) << 2) | (((r32 >> 3) & 1) << 1) | ((r32 >> 1) & 1);
#pragma unroll
      for (int g = 0; g < 4; ++g) {
        const u32 o = io + (((2 * g + h) ^ f) * 8);
        LD64(o) = v2u{ph[g >> 1][(g & 1) * 2], ph[g >> 1][(g & 1) * 2 + 1]};
        LD64(o + 8192) = v2u{ph[2 + (g >> 1)][(g & 1) * 2], ph[2 + (g >> 1)][(g & 1) * 2 + 1]};
        LD64(o + 16384) = v2u{vv[2 * g], vv[2 * g + 1]};
      }
    }
    bar();
    STAMP(6);
    v4u phq[4];
    {
      f16v aQ;
#pragma unroll
      for (int i = 0; i < 16; ++i) aQ[i] = 0.f;
#pragma unroll
      for (int k = 0; k < 28; ++k) aQ = mfma32(LD128(L_WP + 57344 + k * 1024 + lane * 16), xf[k], aQ);
      aQ = mfma32(LD128(L_EXT + 2048 + lane * 16), xe, aQ);
      feature_map(aQ, rl, phq);
    }
    STAMP(7);
    bar();
    {
      v4u g[8];
      for (u32 sp = 0; sp < SPIN_CAP; ++sp) {
        order();
        bool ok = true;
#pragma unroll
        for (int n = 0; n < 8; ++n) g[n] = gld(rW, lane * 16 + n * 1024, WS_KV2 + head * 8192);
#pragma unroll
        for (int n = 0; n < 8; ++n) ok = ok && g[n].y == tag && g[n].w == tag;
        if (__all(ok)) break;
      }
      STAMP(8);
      f16v aO;
#pragma unroll
      for (int i = 0; i < 16; ++i) aO[i] = 0.f;
#pragma unroll
      for (int n = 0; n < 4; ++n) aO = mfma32(v4u{g[2 * n].x, g[2 * n].z, g[2 * n + 1].x, g[2 * n + 1].z}, phq[n], aO);
      const float id = 1.f / (aO[14] + 1e-6f);
      const v4u o0 = v4u{pk(aO[0] * id, aO[1] * id), pk(aO[2] * id, aO[3] * id), pk(aO[4] * id, aO[5] * id), pk(aO[6] * id, aO[7] * id)};
      const v4u o1 = v4u{pk(aO[8] * id, aO[9] * id), pk(aO[10] * id, aO[11] * id), pk(aO[12] * id, aO[13] * id), 0u};
      const u32 vo = (r32 >> 4) * 16384 + h * 256 + (r32 & 15) * 16, so = WS_O + ((mt * 8 + w * 2) * 16 + head) * 1024;
      gst(o0, rW, vo, so);
      gst(o1, rW, vo + 512, so);
      vmwait<0>();
      gst1(tag, rW, lane == 0 ? 0 : OOB, WS_FO + ((mt * 4 + w) * 16 + head) * 4);
      STAMP(9);
    }
    if (pgu) {
      float u1 = 0.f, u2 = 0.f;
#pragma unroll
      for (int e = 0; e < 4; ++e) {
        const v4u s = LD128(L_VEC + 768 + e * 32 + h * 16), t = LD128(L_VEC + 896 + e * 32 + h * 16);
#pragma unroll
        for (int n = 0; n < 4; ++n) { u1 = dot2(pf[e][n], s[n], u1); u2 = dot2(pf[e][n], t[n], u2); }
      }
      u1 = sum32(u1); u2 = sum32(u2);
      gst(v4u{f2u(u1), tag, f2u(u2), tag}, rW, h == 0 ? r32 * 16 : OOB, WS_UG + ((pfb * 4 + w) * 8 + pks) * 512);
    }
    bar();
  } else {
    {
      float t = 0.f;
#pragma unroll
      for (int n = 0; n < 7; ++n) t += u2f(LD32(L_GS + n * 256 + (wi * 16 + (lane & 15)) * 4));
      const u32 so = gbig ? WS_MG0 + (((gblk / 7) * 2 + gk) * 448 + (gblk % 7) * 64) * 8
                          : WS_MG1 + ((((gblk - 14) / 7) * 8 + gk) * 448 + (gblk % 7) * 64) * 8;
      gst2(v2u{f2u(t), tag}, rW, (lane < 16 && b < 252) ? (wi * 16 + lane) * 8 : OOB, so);
      order();
    }
    STAMP(3);
    v4u pB[2], iB[2];
    {
      const int fi = fidx(r32);
#pragma unroll
      for (int s = 0; s < 2; ++s)
#pragma unroll
        for (int n = 0; n < 4; ++n) {
          const int d = 16 * s + 8 * h + 2 * n;
          const bool ok = fi >= 0 && d < 28;
          const u32 o = L_PHI + (ok ? d * 56 + fi * 2 : 0);
          const u32 v = (u32)LD16(o) | ((u32)LD16(o + 56) << 16);
          pB[s][n] = ok ? v : 0u;
          iB[s][n] = (fi == d ? 0x3f80u : 0u) | (fi == d + 1 ? 0x3f800000u : 0u);
        }
    }
    const int rowp = (r32 & 19) | ((r32 & 4) << 1) | ((r32 & 8) >> 1);
    const u32 ro = rowp * 64, rsw = (rowp >> 2) & 3;
    f16v T[4][2], zero;
#pragma unroll
    for (int i = 0; i < 16; ++i) zero[i] = 0.f;
#pragma unroll
    for (int q = 0; q < 4; ++q) {
      const int rho = (q + 3) & 3, kb = wi + 4 * rho;
      if (q == 0) vmwait<13>(); else if (q == 1) vmwait<9>(); else if (q == 2) vmwait<5>(); else vmwait<1>();
      T[rho][0] = zero; T[rho][1] = zero;
      if (rho < 3 || wi < 2) {
#pragma unroll
        for (int s = 0; s < 2; ++s) {
          const u32 o = L_WP + kb * 2048 + ro + (((2 * s + h) ^ rsw) * 16);
          T[rho][0] = mfma32(LD128(o), pB[s], T[rho][0]);
          T[rho][1] = mfma32(LD128(o + 28672), iB[s], T[rho][1]);
        }
      }
    }
    STAMP(4);
    {
      v4u g[4];
      const u32 vo = ((lane >> 4) * 448 + (lane & 15) * 2) * 8;
      for (u32 sp = 0; sp < SPIN_CAP; ++sp) {
        order();
        bool ok = true;
#pragma unroll
        for (int rho = 0; rho < 4; ++rho) g[rho] = gld(rW, vo, WS_MG0 + ((rho < 3 || wi < 2) ? wi + 4 * rho : wi) * 256);
#pragma unroll
        for (int rho = 0; rho < 4; ++rho) ok = ok && g[rho].y == tag && g[rho].w == tag;
        if (__all(ok)) break;
      }
#pragma unroll
      for (int rho = 0; rho < 4; ++rho) {
        float v0 = rbf(sum16(u2f(g[rho].x))), v1 = rbf(sum16(u2f(g[rho].z)));
        if (h) { v0 = rbf(1.f + v0); v1 = rbf(1.f + v1); }
        LD64(L_SB + wi * 1024 + rho * 256 + h * 128 + (lane & 15) * 8) = v2u{f2u(v0), f2u(v1)};
      }
      lgwait();
    }
    STAMP(5);
    float cs_[3] = {0.f, 0.f, 0.f}, bi_[3] = {0.f, 0.f, 0.f};
    auto scale_tile = [&](const f16v& t, const float (&sv)[16], const float (&bv)[16], int part, int kb) {
      v4u o[2];
#pragma unroll
      for (int i = 0; i < 8; ++i) {
        const u32 v = pk(t[2 * i] * sv[2 * i], t[2 * i + 1] * sv[2 * i + 1]);
        o[i >> 2][i & 3] = v;
        cs_[part] = dot2(v, 0x3f803f80u, cs_[part]);
        bi_[part] = fmaf(t[2 * i], bv[2 * i], fmaf(t[2 * i + 1], bv[2 * i + 1], bi_[part]));
      }
      LD128(L_WP + part * 28672 + kb * 2048 + lane * 16) = o[0];
      LD128(L_WP + part * 28672 + kb * 2048 + 1024 + lane * 16) = o[1];
    };
    auto load_sb = [&](int rho, float (&sv)[16], float (&bv)[16]) {
#pragma unroll
      for (int q = 0; q < 4; ++q) {
        const u32 o = L_SB + wi * 1024 + rho * 256 + (q >> 1) * 64 + h * 32 + (q & 1) * 16;
        const f4v bb = LDF4(o), ss = LDF4(o + 128);
#pragma unroll
        for (int n = 0; n < 4; ++n) { bv[4 * q + n] = bb[n]; sv[4 * q + n] = ss[n]; }
      }
    };
    auto put_ext = [&](int part) {
      const float c = sum32(cs_[part]), d = sum32(bi_[part]);
      if (h == 0) LD64(L_EXT + part * 1024 + ((wi >> 1) * 32 + r32) * 16 + (wi & 1) * 8) = v2u{hilo(c), hilo(d)};
    };
#pragma unroll
    for (int rho = 0; rho < 4; ++rho) {
      if (rho < 3 || wi < 2) {
        float sv[16], bv[16];
        load_sb(rho, sv, bv);
        scale_tile(T[rho][0], sv, bv, 0, wi + 4 * rho);
        scale_tile(T[rho][1], sv, bv, 1, wi + 4 * rho);
      }
      if (rho == 3) { put_ext(0); put_ext(1); }
      bar();
      if (rho == 0) {
#pragma unroll
        for (int r2 = 0; r2 < 4; ++r2)
          if (r2 < 3 || wi < 2) {
#pragma unroll
            for (int u = 0; u < 2; ++u)
              dma(LB + L_WP + 57344 + (wi + 4 * r2) * 2048 + u * 1024, vraw, rQ, ((wi + 4 * r2) * 32 + u * 16) * 2688 + head * 56);
          }
      }
    }
    STAMP(6);
    vmwait<0>();
#pragma unroll
    for (int rho = 0; rho < 4; ++rho) {
      if (rho < 3 || wi < 2) {
        const int kb = wi + 4 * rho;
        f16v t = zero;
#pragma unroll
        for (int s = 0; s < 2; ++s) t = mfma32(LD128(L_WP + 57344 + kb * 2048 + ro + (((2 * s + h) ^ rsw) * 16)), pB[s], t);
        float sv[16], bv[16];
        load_sb(rho, sv, bv);
        scale_tile(t, sv, bv, 2, kb);
      }
    }
    put_ext(2);
    STAMP(7);
    bar();
    {
      const int q = lane >> 4, a_ = (lane >> 2) & 3, p = lane & 3;
      f4v kv0 = {0.f, 0.f, 0.f, 0.f}, kv1 = {0.f, 0.f, 0.f, 0.f};
      const int row = 4 * q + a_;
      const u32 f = (((row >> 2) & 1) << 2) | (((row >> 3) & 1) << 1) | ((row >> 1) & 1);
      const u32 oa = L_IMG + (wi >> 1) * 8192 + row * 64 + ((((wi & 1) * 4 + p) ^ f) * 8);
      const u32 ob0 = L_IMG + 16384 + row * 64 + ((p ^ f) * 8), ob1 = L_IMG + 16384 + row * 64 + (((4 + p) ^ f) * 8);
#pragma unroll
      for (int ks = 0; ks < 4; ++ks) {
        const v2u a0 = LDTR(oa + ks * 2048), a1 = LDTR(oa + ks * 2048 + 1024);
        const v2u b0 = LDTR(ob0 + ks * 2048), b1 = LDTR(ob0 + ks * 2048 + 1024);
        const v2u c0 = LDTR(ob1 + ks * 2048), c1 = LDTR(ob1 + ks * 2048 + 1024);
        const v4u A = v4u{a0.x, a0.y, a1.x, a1.y};
        kv0 = mfma16(A, v4u{b0.x, b0.y, b1.x, b1.y}, kv0);
        kv1 = mfma16(A, v4u{c0.x, c0.y, c1.x, c1.y}, kv1);
      }
      const u32 vo = q * 16384 + (lane & 15) * 16, so = WS_KV1 + ((head * 16 + wi * 4) * 16 + mt) * 1024;
      gst(v4u{f2u(kv0[0]), tag, f2u(kv0[1]), tag}, rW, vo, so);
      gst(v4u{f2u(kv1[0]), tag, f2u(kv1[1]), tag}, rW, vo + 256, so);
      gst(v4u{f2u(kv0[2]), tag, f2u(kv0[3]), tag}, rW, vo + 512, so);
      gst(v4u{f2u(kv1[2]), tag, f2u(kv1[3]), tag}, rW, vo + 768, so);
      order();
    }
    STAMP(8);
    if (wi == 0) {
      v4u g[16];
      for (u32 sp = 0; sp < SPIN_CAP; ++sp) {
        order();
        bool ok = true;
#pragma unroll
        for (int n = 0; n < 16; ++n) g[n] = gld(rW, lane * 16, WS_KV1 + ((head * 16 + mt) * 16 + n) * 1024);
#pragma unroll
        for (int n = 0; n < 16; ++n) ok = ok && g[n].y == tag && g[n].w == tag;
        if (__all(ok)) break;
      }
      float s0 = 0.f, s1 = 0.f;
#pragma unroll
      for (int n = 0; n < 16; ++n) { s0 += u2f(g[n].x); s1 += u2f(g[n].z); }
      gst2(v2u{pk(s0, s1), tag}, rW, r32 * 16 + h * 8, WS_KV2 + head * 8192 + mt * 512);
      order();
    } else {
      auto fetch_vec = [&](int vec, int blk, float& v0, float& v1) {
        v4u g[4];
        for (u32 sp = 0; sp < SPIN_CAP; ++sp) {
          order();
          bool ok = true;
#pragma unroll
          for (int n = 0; n < 4; ++n) g[n] = gld(rW, h * 3584 + r32 * 16, WS_MG1 + ((vec * 8 + 2 * n) * 448 + blk * 64) * 8);
#pragma unroll
          for (int n = 0; n < 4; ++n) ok = ok && g[n].y == tag && g[n].w == tag;
          if (__all(ok)) break;
        }
        v0 = rbf(sum32((u2f(g[0].x) + u2f(g[1].x)) + (u2f(g[2].x) + u2f(g[3].x))));
        v1 = rbf(sum32((u2f(g[0].z) + u2f(g[1].z)) + (u2f(g[2].z) + u2f(g[3].z))));
        if (vec == 2) { v0 = rbf(1.f + v0); v1 = rbf(1.f + v1); }
      };
      float v0, v1;
      if (ffn) {
        fetch_vec(wi == 1 ? 0 : wi, cs, v0, v1);
        if (h == 0) LD64(L_VEC + (wi - 1) * 256 + r32 * 8) = v2u{f2u(v0), f2u(v1)};
      }
      if (pgu && wi < 3) {
        fetch_vec(wi, pks, v0, v1);
        if (h == 0) LD32(L_VEC + (wi == 1 ? 896 : 768) + r32 * 4) = pk(v0, v1);
      }
    }
    STAMP(9);
    bar();
    if (ffn) {
      poll_flags(rW, WS_FWO + (cs * 16 + wi * 4) * 4, lane, 4, tag);
#pragma unroll
      for (int n = 0; n < 16; ++n) dma_sc1(LB + L_XR + (wi * 16 + n) * 1024, lane * 16, rW, WS_WOT + (cs * 64 + wi * 16 + n) * 1024);
#pragma unroll
      for (int n = 0; n < 2; ++n) dma(LB + L_X1 + (wi * 2 + n) * 1024, (lane >> 3) * 896 + (lane & 7) * 16, rX, (tt64 * 64 + wi * 16 + n * 8) * 896 + cs * 128);
    } else if (b < 28) {
      const rsrc_t rD = mk(pwd, 1792 * 448 * 2);
#pragma unroll
      for (int n = 0; n < 14; ++n) dma(LB + L_XR + (wi * 14 + n) * 1024, lane * 16, rD, b * 57344 + (wi * 14 + n) * 1024);
    }
    vmwait<0>();
    STAMP(10);
    bar();
  }

  if (!ffn) {
    if (b < 28) {
      const int g = lane >> 4, a_ = (lane >> 2) & 3, p = lane & 3, e = w >> 1, ct = w & 1;
      const u32 o = L_XR + (16 * e + 4 * (g >> 1) + a_) * 896 + ct * 64 + (g & 1) * 32 + p * 8;
#pragma unroll
      for (int c = 0; c < 7; ++c) {
        const v2u t0 = LDTR(o + c * 128), t1 = LDTR(o + c * 128 + 8 * 896);
        gst(v4u{t0.x, t0.y, t1.x, t1.y}, rW, lane * 16, WS_WDT + ((c * 2 + ct) * 112 + b * 4 + e) * 1024);
      }
      vmwait<0>();
      bar();
      if (w == 0) gst1(tag, rW, lane == 0 ? 0 : OOB, WS_FWD + b * 4);
    }
    STAMP_FLUSH();
    return;
  }

  const int ft = fs * 8 + w;
  auto load_w = [&]() {
    poll_flags(rW, WS_FW + ft * 32, lane, 7, tag);
#pragma unroll
    for (int k = 0; k < 28; ++k) Wf[k] = gld(rW, lane * 16 + (k & 3) * 1024, WS_WGT + (ft * 28 + (k & ~3)) * 1024);
  };
  if (w < 4) {
    load_w();
  } else {
    const int tt16 = tt64 * 4 + wi, q = lane >> 4, tk = lane & 15;
    poll_flags(rW, WS_FO + (tt16 >> 1) * 64, lane, 16, tag);
    STAMP(11);
    v4u of[16];
#pragma unroll
    for (int n = 0; n < 16; ++n) of[n] = gld(rW, lane * 16 + (n & 3) * 1024, WS_O + (tt16 * 16 + (n & ~3)) * 1024);
    if (b == 32 && w == 4) gst1(tag, rW, lane == 0 ? 0 : OOB, WS_EPOCH);
    f4v ac[4];
#pragma unroll
    for (int t = 0; t < 4; ++t) ac[t] = f4v{0.f, 0.f, 0.f, 0.f};
#pragma unroll
    for (int n = 0; n < 16; ++n)
#pragma unroll
      for (int t = 0; t < 4; ++t) ac[t] = mfma16(LD128(L_XR + (n * 4 + t) * 1024 + lane * 16), of[n], ac[t]);
    STAMP(12);
    const u32 xo = L_X1 + (wi * 16 + tk) * 128 + q * 32;
    const v4u xa = LD128(xo), xb = LD128(xo + 16);
    float x1[16], sm = 0.f;
#pragma unroll
    for (int t = 0; t < 4; ++t) {
      const f4v ga = LDF4(L_VEC + q * 64 + t * 16);
      const u32 w0 = t < 2 ? xa[2 * t] : xb[2 * t - 4], w1 = t < 2 ? xa[2 * t + 1] : xb[2 * t - 3];
      x1[4 * t] = fmaf(ga[0], ac[t][0], lo2f(w0)); x1[4 * t + 1] = fmaf(ga[1], ac[t][1], hi2f(w0));
      x1[4 * t + 2] = fmaf(ga[2], ac[t][2], lo2f(w1)); x1[4 * t + 3] = fmaf(ga[3], ac[t][3], hi2f(w1));
      sm += (x1[4 * t] + x1[4 * t + 1]) + (x1[4 * t + 2] + x1[4 * t + 3]);
    }
    const float ml = sum32(sum16(sm)) * (1.f / 64.f), mr = rbf(ml);
    float m2 = 0.f;
    u32 yv[8];
#pragma unroll
    for (int t = 0; t < 4; ++t) {
      const f4v s2 = LDF4(L_VEC + 256 + q * 64 + t * 16);
      const float d0 = x1[4 * t] - mr, d1 = x1[4 * t + 1] - mr, d2 = x1[4 * t + 2] - mr, d3 = x1[4 * t + 3] - mr;
      m2 += (d0 * d0 + d1 * d1) + (d2 * d2 + d3 * d3);
      yv[2 * t] = pk(d0 * s2[0], d1 * s2[1]); yv[2 * t + 1] = pk(d2 * s2[2], d3 * s2[3]);
    }
    const u32 vo = q * 1024 + ((tt16 & 1) * 16 + tk) * 16, so = WS_Y + ((tt16 >> 1) * 28 + cs * 4) * 1024;
    gst(v4u{yv[0], yv[1], yv[2], yv[3]}, rW, vo, so);
    gst(v4u{yv[4], yv[5], yv[6], yv[7]}, rW, vo + 512, so);
    m2 = sum32(sum16(m2)) - 64.f * (ml - mr) * (ml - mr);
    gst(v4u{f2u(ml), tag, f2u(m2), tag}, rW, q == 0 ? ((tt16 & 1) * 16 + tk) * 16 : OOB, WS_ST + ((tt16 >> 1) * 8 + cs) * 512);
    LD128(xo) = v4u{pk(x1[0], x1[1]), pk(x1[2], x1[3]), pk(x1[4], x1[5]), pk(x1[6], x1[7])};
    LD128(xo + 16) = v4u{pk(x1[8], x1[9]), pk(x1[10], x1[11]), pk(x1[12], x1[13]), pk(x1[14], x1[15])};
    vmwait<0>();
    gst1(tag, rW, lane == 0 ? 0 : OOB, WS_FY + (((tt16 >> 3) * 8 + cs) * 8 + (tt16 & 7)) * 4);
    STAMP(13);
    load_w();
  }
  {
    v4u g[4];
    for (u32 sp = 0; sp < SPIN_CAP; ++sp) {
      order();
      bool ok = true;
#pragma unroll
      for (int n = 0; n < 4; ++n) g[n] = gld(rW, (n == 3 ? 3 : 4 * h + n) * 512 + r32 * 16, WS_UG + ft * 4096);
#pragma unroll
      for (int n = 0; n < 4; ++n) ok = ok && g[n].y == tag && g[n].w == tag;
      if (__all(ok)) break;
    }
    const float u2 = sum32(u2f(g[0].z) + u2f(g[1].z) + u2f(g[2].z) + (h ? 0.f : u2f(g[3].z)));
    Wf[28] = v4u{hilo(u2f(g[0].x)), hilo(u2f(g[1].x)), hilo(u2f(g[2].x)), hilo(h ? u2 : u2f(g[3].x))};
  }
#pragma unroll
  for (int k = 0; k < 28; ++k) asm volatile("" : "+v"(Wf[k]));
  STAMP(14);
  bar();

  if (w < 7) {
    poll_flags(rW, WS_FY + (mt3 * 8 + w) * 32, lane, 8, tag);
#pragma unroll
    for (int t = 0; t < 4; ++t)
#pragma unroll
      for (int e = 0; e < 4; ++e) dma_sc1(LB + L_Y + (t * 28 + w * 4 + e) * 1024, lane * 16, rW, WS_Y + ((mt3 * 4 + t) * 28 + w * 4 + e) * 1024);
  } else {
    v4u g[4][4];
    for (u32 sp = 0; sp < SPIN_CAP; ++sp) {
      order();
      bool ok = true;
#pragma unroll
      for (int t = 0; t < 4; ++t)
#pragma unroll
        for (int n = 0; n < 4; ++n) g[t][n] = gld(rW, (n == 3 ? 3 : 4 * h + n) * 512 + r32 * 16, WS_ST + (mt3 * 4 + t) * 4096);
#pragma unroll
      for (int t = 0; t < 4; ++t)
#pragma unroll
        for (int n = 0; n < 4; ++n) ok = ok && g[t][n].y == tag && g[t][n].w == tag;
      if (__all(ok)) break;
    }
#pragma unroll
    for (int t = 0; t < 4; ++t) {
      const float m0 = u2f(g[t][0].x), m1 = u2f(g[t][1].x), m2 = u2f(g[t][2].x), m3 = u2f(g[t][3].x);
      const float mean = sum32(m0 + m1 + m2 + (h ? 0.f : m3)) * (1.f / 7.f);
      const float d0 = m0 - mean, d1 = m1 - mean, d2 = m2 - mean, d3 = m3 - mean;
      const float part = u2f(g[t][0].z) + u2f(g[t][1].z) + u2f(g[t][2].z) + 64.f * (d0 * d0 + d1 * d1 + d2 * d2) + (h ? 0.f : u2f(g[t][3].z) + 64.f * d3 * d3);
      const float var = fmaxf(sum32(part) * (1.f / 448.f), 0.f);
      const float sig = rbf(__builtin_sqrtf(rbf(rbf(var) + 1e-5f))), mr = rbf(mean);
      const float e0 = rbf(m0) - mr, e1 = rbf(m1) - mr, e2 = rbf(m2) - mr, e3 = h ? sig : rbf(m3) - mr;
      LD128(L_YEXT + t * 1024 + lane * 16) = v4u{pk(e0, e0), pk(e1, e1), pk(e2, e2), pk(e3, e3)};
      if (h == 0) LD32(L_SIG + (t * 32 + r32) * 4) = f2u(sig);
    }
  }
  STAMP(15);
#pragma unroll
  for (int t = 0; t < 4; ++t) {
    if (t == 0) vmwait<12>(); else if (t == 1) vmwait<9>(); else if (t == 2) vmwait<7>(); else vmwait<5>();
    bar();
    if (t == 0) STAMP(16);
    f16v ac;
#pragma unroll
    for (int i = 0; i < 16; ++i) ac[i] = 0.f;
#pragma unroll
    for (int k = 0; k < 28; ++k) ac = mfma32(Wf[k], LD128(L_Y + (t * 28 + k) * 1024 + lane * 16), ac);
    ac = mfma32(Wf[28], LD128(L_YEXT + t * 1024 + lane * 16), ac);
    if (t > 0) {
      vmwait<0>();
      gst1(tag, rW, lane == 0 ? 0 : OOB, WS_FA + ((mt3 * 4 + t - 1) * 128 + ft) * 4);
      order();
    }
    const float sig = u2f(LD32(L_SIG + (t * 32 + r32) * 4));
    const float r = 1.f / sig, s2 = sig * sig;
    const float k0 = -2.302208198f * r, k1 = -0.1029432397f * r * r * r;
    float av[8];
#pragma unroll
    for (int i = 0; i < 8; ++i) {
      const float g = ac[i], z = g * fmaf(g * g, k1, k0);
      av[i] = (g * ac[8 + i]) * __builtin_amdgcn_rcpf(fmaf(__builtin_amdgcn_exp2f(z), s2, s2));
    }
    gst(v4u{pk(av[0], av[1]), pk(av[2], av[3]), pk(av[4], av[5]), pk(av[6], av[7])}, rW, lane * 16, WS_ACT + ((mt3 * 4 + t) * 112 + ft) * 1024);
    order();
  }
  vmwait<0>();
  gst1(tag, rW, lane == 0 ? 0 : OOB, WS_FA + ((mt3 * 4 + 3) * 128 + ft) * 4);
  STAMP(17);

  const int ct = w & 1, kq = w >> 1;
  poll_flags(rW, WS_FWD, lane, 28, tag);
  v4u Wd[28];
#pragma unroll
  for (int n = 0; n < 28; ++n) Wd[n] = gld(rW, lane * 16 + (n & 1) * 1024, WS_WDT + ((cs * 2 + ct) * 112 + (n >> 1) * 8 + kq * 2) * 1024);
  {
    const u32 vo = (lane < 28 ? (lane >> 1) * 8 + kq * 2 + (lane & 1) : kq * 2) * 4;
    for (u32 sp = 0; sp < SPIN_CAP; ++sp) {
      order();
      const u32 v = gld1(rW, vo, WS_FA + (tt64 * 2 + ct) * 512);
      if (__all(v == tag)) break;
    }
    order();
  }
#pragma unroll
  for (int n = 0; n < 28; ++n) asm volatile("" : "+v"(Wd[n]));
  STAMP(18);
  bar();
  const u32 asrc = WS_ACT + ((tt64 * 2 + ct) * 112 + kq * 2) * 1024, adst = LB + (kq * 4 + ct * 2) * 1024;
#pragma unroll
  for (int s = 0; s < 8; ++s) {
    dma_sc1(adst + s * 16384, lane * 16, rW, asrc + s * 8192);
    dma_sc1(adst + s * 16384 + 1024, lane * 16, rW, asrc + s * 8192 + 1024);
  }
  f16v d0, d1;
#pragma unroll
  for (int i = 0; i < 16; ++i) { d0[i] = 0.f; d1[i] = 0.f; }
#pragma unroll
  for (int sp = 0; sp < 7; ++sp) {
    if (sp == 0) vmwait<12>(); else if (sp < 5) vmwait<8>(); else if (sp == 5) vmwait<4>(); else vmwait<0>();
    bar();
    if (sp >= 1 && sp <= 3) {
#pragma unroll
      for (int s = 2 * sp + 6; s < 2 * sp + 8; ++s) {
        dma_sc1(adst + (s & 7) * 16384, lane * 16, rW, asrc + s * 8192);
        dma_sc1(adst + (s & 7) * 16384 + 1024, lane * 16, rW, asrc + s * 8192 + 1024);
      }
    }
#pragma unroll
    for (int s = 2 * sp; s < 2 * sp + 2; ++s)
#pragma unroll
      for (int e = 0; e < 2; ++e) {
        const u32 o = (s & 7) * 16384 + (kq * 4 + e) * 1024 + lane * 16;
        d0 = mfma32(Wd[2 * s + e], LD128(o), d0);
        d1 = mfma32(Wd[2 * s + e], LD128(o + 2048), d1);
      }
  }
  STAMP(19);
  bar();
  {
    const u32 o = kq * 17408 + r32 * 272 + ct * 128 + h * 16;
#pragma unroll
    for (int g = 0; g < 4; ++g) {
      LDF4(o + g * 32) = f4v{d0[4 * g], d0[4 * g + 1], d0[4 * g + 2], d0[4 * g + 3]};
      LDF4(o + 32 * 272 + g * 32) = f4v{d1[4 * g], d1[4 * g + 1], d1[4 * g + 2], d1[4 * g + 3]};
    }
  }
  bar();
  {
    const int tk = w * 8 + (lane >> 3), c8 = lane & 7;
    f4v a = {0.f, 0.f, 0.f, 0.f}, c = {0.f, 0.f, 0.f, 0.f};
#pragma unroll
    for (int n = 0; n < 4; ++n) { a += LDF4(n * 17408 + tk * 272 + c8 * 32); c += LDF4(n * 17408 + tk * 272 + c8 * 32 + 16); }
    const v4u x1 = LD128(L_X1 + tk * 128 + c8 * 16);
    const f4v g0 = LDF4(L_VEC + 512 + c8 * 32), g1 = LDF4(L_VEC + 512 + c8 * 32 + 16);
    const v4u o = v4u{pk(fmaf(g0[0], a[0], lo2f(x1[0])), fmaf(g0[1], a[1], hi2f(x1[0]))), pk(fmaf(g0[2], a[2], lo2f(x1[1])), fmaf(g0[3], a[3], hi2f(x1[1]))),
                      pk(fmaf(g1[0], c[0], lo2f(x1[2])), fmaf(g1[1], c[1], hi2f(x1[2]))), pk(fmaf(g1[2], c[2], lo2f(x1[3])), fmaf(g1[3], c[3], hi2f(x1[3])))};
    __builtin_amdgcn_raw_buffer_store_b128(o, mk(pout, S_ * D_ * 2), (lane >> 3) * 896 + c8 * 16, (tt64 * 64 + w * 8) * 896 + cs * 128, 0);
  }
  STAMP(20);
  STAMP_FLUSH();
}

extern "C" void kernel_launch(void* const* d_in, const int* in_sizes, int n_in, void* d_out, int out_size, void* d_ws, size_t ws_size,
                              hipStream_t stream) {
  fused_block<<<256, 512, 0, stream>>>(d_in[0], d_in[1], d_in[2], d_in[3], d_in[4], d_in[5], d_in[6], d_in[7], d_in[8], d_in[9], d_out, d_ws);
}
```

```cpp
#include <hip/hip_runtime.h>
__global__ void __launch_bounds__(512, 2)
fused_block(const void*, const void*, const void*, const void*, const void*, const void*, const void*, const void*, const void*, const void*,
            void*, void*) {
  __shared__ __attribute__((aligned(16))) char lds[163840];
  asm volatile(R"ASM(
  s_load_dwordx16 s[24:39], s[0:1], 0
  s_load_dwordx8 s[40:47], s[0:1], 0x40
  v_lshrrev_b32 v1, 6, v0
  v_and_b32 v0, 63, v0
  v_readfirstlane_b32 s48, v1
  s_and_b32 s55, s2, 7
  s_lshr_b32 s56, s2, 3
  v_lshlrev_b32 v1, 4, v0
  v_mov_b32 v4, 2146435072
  s_mov_b32 s15, 0x20000
  s_mov_b32 s19, 0x20000
  s_cmp_ge_u32 s56, 14
  s_cbranch_scc1 .Lnogemv
  s_mul_i32 s59, s55, 7
  s_add_u32 s59, s59, s48
  s_cmp_eq_u32 s48, 7
  s_cselect_b32 s59, 0, s59
  s_cselect_b32 s3, 2146435072, 0
  s_mul_i32 s58, s59, 43008
  s_lshl_b32 s57, s56, 7
  s_add_u32 s58, s58, s57
  s_lshl_b32 s59, s59, 4
  v_lshrrev_b32 v7, 3, v0
  v_and_b32 v8, 0x70, v1
  v_mul_u32_u24 v5, 5376, v7
  v_or3_b32 v5, v5, v8, s3
  v_lshl_or_b32 v6, v7, 1, s3
  s_mov_b32 s14, 2408448
  s_mov_b32 s18, 896
  s_waitcnt lgkmcnt(0)
  s_mov_b32 s16, s40
  s_and_b32 s17, s41, 0xffff
  buffer_load_ushort v12, v6, s[16:19], s59 offen
  s_mov_b32 s12, s42
  s_and_b32 s13, s43, 0xffff
  buffer_load_dwordx4 v[16:19], v5, s[12:15], s58 offen
  s_load_dword s50, s[46:47], 0
  s_and_b32 s51, s55, 6
  s_and_b32 s57, s56, 3
  s_lshl1_add_u32 s51, s51, s57
  s_and_b32 s52, s55, 1
  s_lshr_b32 s23, s56, 2
  s_lshl3_add_u32 s52, s52, s23
  s_lshl1_add_u32 s23, s57, s23
  s_sub_u32 s57, s23, 7
  s_cselect_b32 s23, s23, s57
  s_and_b32 s49, s48, 3
  v_and_b32 v3, 31, v0
  v_lshlrev_b32 v3, 2, v3
  v_mov_b32 v8, 161024
  v_mov_b32 v9, 0
  ds_write_b32 v8, v9
  s_mov_b32 s4, s46
  s_and_b32 s5, s47, 0xffff
  s_mov_b32 s6, 21581824
  s_mov_b32 s7, 0x20000
  s_cmp_ge_u32 s48, 4
  s_cbranch_scc1 .Lprep0
  s_mov_b32 s8, s24
  s_and_b32 s9, s25, 0xffff
  s_mov_b32 s10, 1835008
  s_mov_b32 s11, 0x20000
  v_lshrrev_b32 v80, 3, v0
  v_mul_u32_u24 v80, 896, v80
  v_lshrrev_b32 v81, 4, v0
  v_xor_b32 v81, v81, v0
  v_and_b32 v81, 7, v81
  v_lshl_or_b32 v80, v81, 4, v80
  v_xor_b32 v81, 64, v80
  v_add_u32 v81, 6144, v81
  v_add_u32 v82, 12288, v80
  v_add_u32 v83, 12288, v81
  s_lshl2_add_u32 s0, s51, s48
  s_mul_i32 s0, s0, 28672
  s_mul_i32 s1, s48, 28672
  s_mov_b32 m0, s1
  s_add_u32 s33, s0, 896
  s_lshl_b32 s57, s23, 7
  s_add_u32 s0, s0, s57
  s_branch .Lgemv
.Lprep0:
  s_setprio 1
  s_mov_b32 s43, 0
  s_mul_i32 s33, s2, 9363
  s_lshr_b32 s33, s33, 16
  s_mul_i32 s58, s33, 7
  s_sub_u32 s32, s2, s58
  v_lshrrev_b32 v57, 5, v0
  v_bfe_u32 v42, v0, 2, 2
  v_and_b32 v43, 3, v0
  v_bfe_u32 v44, v0, 4, 1
  s_cmp_lt_u32 s49, 2
  s_cselect_b32 s12, s34, s36
  s_cselect_b32 s13, s35, s37
  s_cselect_b32 s57, 0, 128
  s_and_b32 s13, s13, 0xffff
  s_mov_b32 s14, 1605632
  v_lshrrev_b32 v40, 4, v0
  v_lshlrev_b32 v41, 5, v40
  v_xor_b32 v41, v41, v1
  v_and_b32 v41, 0xf0, v41
  v_xor_b32 v41, s57, v41
  v_mul_u32_u24 v40, 3584, v40
  v_or_b32 v40, v40, v41
  s_and_b32 s57, s49, 1
  s_lshl1_add_u32 s57, s32, s57
  s_mul_i32 s57, s57, 114688
  s_lshl_b32 s58, s33, 8
  s_add_u32 s57, s57, s58
  s_lshl_b32 s58, s49, 13
  s_add_u32 m0, s58, 114688
  s_mov_b32 s0, s57
  s_movk_i32 s8, 13312
  v_lshl_add_u32 v48, v57, 3, v42
  v_lshlrev_b32 v48, 8, v48
  v_lshl_add_u32 v48, v44, 14, v48
  v_lshl_add_u32 v48, v43, 3, v48
  v_lshl_or_b32 v49, v44, 2, v42
  s_lshl_b32 s57, s49, 1
  v_xor_b32 v49, s57, v49
  v_lshl_add_u32 v48, v49, 5, v48
  v_add_u32 v48, 114688, v48
  v_add_u32 v49, 1024, v48
  v_xor_b32 v50, 32, v48
  v_xor_b32 v51, 32, v49
  s_lshl3_add_u32 s57, s33, s57
  s_mul_i32 s20, s57, 7
  s_add_u32 s20, s20, s32
  s_lshl_b32 s20, s20, 12
  s_add_u32 s20, s20, 9424896
  s_add_u32 s21, s20, 28672
  s_lshl3_add_u32 s22, s57, s32
  s_lshl_b32 s22, s22, 2
  s_add_u32 s22, s22, 4096
.Lprep2:
.Lgemv:
  v_and_b32 v10, 7, v0
  v_lshlrev_b32 v10, 5, v10
  v_lshrrev_b32 v11, 4, v0
  v_lshl_or_b32 v10, v11, 3, v10
  s_lshl_b32 s57, s48, 8
  s_add_u32 s57, s57, 154624
  v_add_u32 v10, s57, v10
  v_lshlrev_b32 v11, 2, v0
  v_add_u32 v11, 154624, v11
  s_cmp_ge_u32 s56, 7
  s_cselect_b32 s57, 49, 0
  s_mul_i32 s58, s55, 7
  s_add_u32 s57, s57, s58
  s_add_u32 s57, s57, s56
  s_lshl_b32 s57, s57, 9
  s_add_u32 s58, s57, 1306624
  v_lshlrev_b32 v14, 3, v0
  s_cmp_eq_u32 s48, 7
  s_cbranch_scc1 .Lw7
  v_mov_b32 v24, 0
  v_mov_b32 v25, 0
  v_mov_b32 v26, 0
  v_mov_b32 v27, 0
  v_mov_b32 v28, 0
  v_mov_b32 v29, 0
  v_mov_b32 v30, 0
  v_mov_b32 v31, 0
  s_waitcnt lgkmcnt(0)
  s_mul_i32 s50, s50, 1664525
  s_add_u32 s50, s50, 1013904223
  v_mov_b32 v2, s50
  s_waitcnt vmcnt(1)
  v_lshlrev_b32 v13, 16, v12
  v_mul_f32 v20, 0xbfb8aa3b, v13
  v_exp_f32 v20, v20
  s_nop 0
  v_add_f32 v20, 1.0, v20
  v_rcp_f32 v20, v20
  s_nop 0
  v_mul_f32 v13, v13, v20
  v_cvt_pk_bf16_f32 v20, v13, 0
  v_cvt_pk_bf16_f32 v21, 0, v13
  s_waitcnt vmcnt(0)
  v_dot2c_f32_bf16 v24, v16, v20
  v_dot2c_f32_bf16 v25, v16, v21
  v_dot2c_f32_bf16 v26, v17, v20
  v_dot2c_f32_bf16 v27, v17, v21
  v_dot2c_f32_bf16 v28, v18, v20
  v_dot2c_f32_bf16 v29, v18, v21
  v_dot2c_f32_bf16 v30, v19, v20
  v_dot2c_f32_bf16 v31, v19, v21
  v_permlane32_swap_b32 v24, v28
  v_permlane32_swap_b32 v25, v29
  v_permlane32_swap_b32 v26, v30
  v_permlane32_swap_b32 v27, v31
  v_add_f32 v24, v24, v28
  v_add_f32 v25, v25, v29
  v_add_f32 v26, v26, v30
  v_add_f32 v27, v27, v31
  s_nop 0
  v_permlane16_swap_b32 v24, v26
  v_permlane16_swap_b32 v25, v27
  v_add_f32 v24, v24, v26
  v_add_f32 v25, v25, v27
  s_nop 0
  v_add_f32_dpp v24, v24, v24 row_ror:8 row_mask:0xf bank_mask:0xf
  v_add_f32_dpp v25, v25, v25 row_ror:8 row_mask:0xf bank_mask:0xf
  ds_write_b64 v10, v[24:25]
  s_waitcnt lgkmcnt(0)
  s_barrier
  s_branch .Lrole
.Lw7:
  s_mov_b32 s16, s30
  s_and_b32 s17, s31, 0xffff
  s_mov_b32 s18, 401408
  s_mul_i32 s57, s33, 25088
  s_lshl_b32 s59, s32, 7
  s_add_u32 s57, s57, s59
  v_mul_u32_u24 v32, 896, v7
  v_and_b32 v33, 0x70, v1
  v_or_b32 v32, v32, v33
  s_mov_b32 s9, m0
  s_mov_b32 m0, 147456
  s_nop 0
  buffer_load_dwordx4 v32, s[16:19], s57 offen lds
  s_add_u32 s57, s57, 6144
  buffer_load_dwordx4 v32, s[16:19], s57 offen offset:1024 lds
  s_add_u32 s57, s57, 6144
  buffer_load_dwordx4 v32, s[16:19], s57 offen offset:2048 lds
  s_add_u32 s57, s57, 6144
  buffer_load_dwordx4 v32, s[16:19], s57 offen offset:3072 lds
  s_mov_b32 m0, s9
  v_lshl_add_u32 v32, v44, 2, v42
  v_lshl_add_u32 v32, v57, 4, v32
  v_add_u32 v33, 8, v32
  v_cmp_lt_u32 vcc, 27, v33
  v_subrev_u32 v34, 2, v33
  v_lshlrev_b32 v35, 5, v43
  v_cndmask_b32 v33, v33, v34, vcc
  v_add_u32 v35, 147456, v35
  v_min_u32 v33, 27, v33
  v_lshl_add_u32 v32, v32, 7, v35
  v_lshl_add_u32 v33, v33, 7, v35
  s_lshl4_add_u32 s43, s32, s33
  s_lshl_b32 s9, s43, 12
  s_add_u32 s9, s9, 2609152
  s_lshl_b32 s43, s43, 2
  s_add_u32 s43, s43, 8192
  s_waitcnt vmcnt(4) lgkmcnt(0)
  s_mul_i32 s50, s50, 1664525
  s_add_u32 s50, s50, 1013904223
  v_mov_b32 v2, s50
  s_barrier
  ds_read2st64_b32 v[16:17], v11 offset0:0 offset1:1
  ds_read2st64_b32 v[18:19], v11 offset0:2 offset1:3
  ds_read2st64_b32 v[20:21], v11 offset0:4 offset1:5
  ds_read_b32 v22, v11 offset:1536
  s_waitcnt lgkmcnt(2)
  v_pk_add_f32 v[16:17], v[16:17], v[18:19]
  s_waitcnt lgkmcnt(0)
  v_add_f32 v20, v20, v22
  v_pk_add_f32 v[16:17], v[16:17], v[20:21]
  v_add_f32 v16, v16, v17
  v_mov_b32 v17, v2
  buffer_store_dwordx2 v[16:17], v14, s[4:7], s58 offen sc1
  s_waitcnt vmcnt(1)
  ds_read_b64_tr_b16 v[100:101], v32
  ds_read_b64_tr_b16 v[102:103], v33
  ds_read_b64_tr_b16 v[104:105], v32 offset:8
  ds_read_b64_tr_b16 v[106:107], v33 offset:8
  ds_read_b64_tr_b16 v[108:109], v32 offset:16
  ds_read_b64_tr_b16 v[110:111], v33 offset:16
  ds_read_b64_tr_b16 v[112:113], v32 offset:24
  ds_read_b64_tr_b16 v[114:115], v33 offset:24
  s_waitcnt lgkmcnt(6)
  buffer_store_dwordx4 v[100:103], v1, s[4:7], s9 offen sc1
  s_waitcnt lgkmcnt(4)
  buffer_store_dwordx4 v[104:107], v1, s[4:7], s9 offen offset:1024 sc1
  s_waitcnt lgkmcnt(2)
  buffer_store_dwordx4 v[108:111], v1, s[4:7], s9 offen offset:2048 sc1
  s_waitcnt lgkmcnt(0)
  buffer_store_dwordx4 v[112:115], v1, s[4:7], s9 offen offset:3072 sc1
.Lrole:
  s_sleep 3
  s_cmp_lt_u32 s48, 4
  s_cbranch_scc0 .Lprep
  s_branch .LA
.Lnogemv:
  s_and_b32 s51, s55, 6
  s_and_b32 s57, s56, 3
  s_lshl1_add_u32 s51, s51, s57
  s_and_b32 s52, s55, 1
  s_lshr_b32 s23, s56, 2
  s_lshl3_add_u32 s52, s52, s23
  s_lshl1_add_u32 s23, s57, s23
  s_sub_u32 s57, s23, 7
  s_cselect_b32 s23, s23, s57
  s_and_b32 s49, s48, 3
  v_and_b32 v3, 31, v0
  v_lshlrev_b32 v3, 2, v3
  v_mov_b32 v8, 161024
  v_mov_b32 v9, 0
  ds_write_b32 v8, v9
  s_cmp_ge_u32 s48, 4
  s_cbranch_scc1 .LB0
  s_waitcnt lgkmcnt(0)
  s_load_dword s50, s[46:47], 0
  s_mov_b32 s4, s46
  s_and_b32 s5, s47, 0xffff
  s_mov_b32 s6, 21581824
  s_mov_b32 s7, 0x20000
  s_mov_b32 s8, s24
  s_and_b32 s9, s25, 0xffff
  s_mov_b32 s10, 1835008
  s_mov_b32 s11, 0x20000
  v_lshrrev_b32 v80, 3, v0
  v_mul_u32_u24 v80, 896, v80
  v_lshrrev_b32 v81, 4, v0
  v_xor_b32 v81, v81, v0
  v_and_b32 v81, 7, v81
  v_lshl_or_b32 v80, v81, 4, v80
  v_xor_b32 v81, 64, v80
  v_add_u32 v81, 6144, v81
  v_add_u32 v82, 12288, v80
  v_add_u32 v83, 12288, v81
  s_lshl2_add_u32 s0, s51, s48
  s_mul_i32 s0, s0, 28672
  s_mul_i32 s1, s48, 28672
  s_mov_b32 m0, s1
  s_add_u32 s33, s0, 896
  s_lshl_b32 s57, s23, 7
  s_add_u32 s0, s0, s57
  s_barrier
  s_sleep 2
  s_branch .LA
.LB0:
  s_cmp_lg_u32 s48, 7
  s_cbranch_scc1 .Lnopf
  s_getpc_b64 s[8:9]
.Lpc:
  s_and_b32 s57, s56, 3
  s_lshl_b32 s57, s57, 13
  v_lshl_add_u32 v5, v0, 7, s57
  v_min_u32 v5, .Lend-.Lpc-4, v5
  s_mov_b32 m0, 161280
  s_nop 0
  global_load_lds_dword v5, s[8:9]
.Lnopf:
  s_setprio 1
  s_cmp_ge_u32 s56, 28
  s_cbranch_scc1 .Lrest
  s_mul_i32 s3, s55, 14
  s_add_u32 s3, s3, s56
  s_sub_u32 s3, s3, 14
  s_lshr_b32 s53, s3, 3
  v_lshrrev_b32 v7, 2, v0
  s_and_b32 s57, s3, 7
  s_and_b32 s58, s48, 1
  s_lshl1_add_u32 s54, s57, s58
  v_lshrrev_b32 v44, 4, v0
  v_xor_b32 v44, v44, v0
  v_and_b32 v44, 3, v44
  v_lshlrev_b32 v44, 4, v44
  v_mul_u32_u24 v45, 2688, v7
  v_or_b32 v44, v44, v45
  s_lshl_b32 s10, s49, 12
  s_add_u32 s10, s10, 114688
  s_mul_i32 s11, s53, 86016
  s_mul_i32 s57, s54, 56
  s_add_u32 s11, s11, s57
  s_lshr_b32 s57, s49, 1
  s_mul_i32 s57, s57, 896
  s_add_u32 s57, s57, s11
  s_add_u32 s57, s57, 896
  s_add_u32 s58, s57, 41984
  s_mul_i32 s59, s54, 1568
  s_mov_b32 s14, 1204224
  s_mov_b32 s18, 25088
  s_add_u32 m0, s10, 16384
  s_waitcnt lgkmcnt(0)
  s_mov_b32 s12, s26
  s_and_b32 s13, s27, 0xffff
  s_cmp_ge_u32 s49, 2
  s_cbranch_scc1 .Lraw
  s_mov_b32 s16, s28
  s_and_b32 s17, s29, 0xffff
  buffer_load_dwordx4 v1, s[16:19], s59 offen lds
  buffer_load_dwordx4 v1, s[16:19], s59 offen offset:1024 lds
.Lraw:
  s_mov_b32 m0, s10
  s_load_dword s50, s[46:47], 0
  buffer_load_dwordx4 v44, s[12:15], s57 offen lds
  buffer_load_dwordx4 v44, s[12:15], s58 offen offset:1024 lds
  s_cmp_ge_u32 s49, 2
  s_cbranch_scc1 .Lnoq
  s_add_u32 m0, s10, 2048
  s_add_u32 s58, s11, 41984
  buffer_load_dwordx4 v44, s[12:15], s11 offen lds
  buffer_load_dwordx4 v44, s[12:15], s58 offen offset:1024 lds
.Lnoq:
  s_barrier
  v_lshrrev_b32 v57, 5, v0
  v_and_b32 v58, 19, v0
  v_bfe_u32 v59, v0, 2, 1
  v_bfe_u32 v60, v0, 3, 1
  v_lshl_or_b32 v59, v59, 1, v60
  v_lshl_or_b32 v58, v59, 2, v58
  v_xor_b32 v59, v59, v57
  v_lshlrev_b32 v59, 4, v59
  v_lshl_or_b32 v58, v58, 6, v59
  v_add_u32 v58, s10, v58
  v_xor_b32 v59, 32, v58
  s_mov_b32 s0, 0x33ffffff
  s_mov_b32 s1, 0x33ffffff
  s_cmp_ge_u32 s49, 2
  s_cbranch_scc1 .Lident
  v_bfe_u32 v56, v0, 2, 2
  v_lshl_add_u32 v56, v57, 3, v56
  v_mul_u32_u24 v56, 56, v56
  v_bfe_u32 v60, v0, 4, 1
  v_and_b32 v61, 3, v0
  v_lshl_add_u32 v60, v60, 2, v61
  v_min_u32 v60, 6, v60
  v_lshl_add_u32 v56, v60, 3, v56
  v_add_u32 v56, s10, v56
  s_waitcnt vmcnt(4)
  ds_read_b64_tr_b16 v[48:49], v56 offset:16384
  ds_read_b64_tr_b16 v[50:51], v56 offset:16608
  ds_read_b64_tr_b16 v[52:53], v56 offset:17280
  ds_read_b64_tr_b16 v[54:55], v56 offset:17504
  s_waitcnt lgkmcnt(0)
  v_mov_b32_dpp v48, v48 row_shr:2 row_mask:0xa bank_mask:0x8
  v_mov_b32_dpp v49, v49 row_shr:2 row_mask:0xa bank_mask:0x8
  v_mov_b32_dpp v50, v50 row_shr:2 row_mask:0xa bank_mask:0x8
  v_mov_b32_dpp v51, v51 row_shr:2 row_mask:0xa bank_mask:0x8
  v_mov_b32_dpp v52, v52 row_shr:2 row_mask:0xa bank_mask:0x8
  v_mov_b32_dpp v53, v53 row_shr:2 row_mask:0xa bank_mask:0x8
  v_mov_b32_dpp v54, v54 row_shr:2 row_mask:0xa bank_mask:0x8
  v_mov_b32_dpp v55, v55 row_shr:2 row_mask:0xa bank_mask:0x8
  v_cndmask_b32 v48, 0, v48, s[0:1]
  v_cndmask_b32 v49, 0, v49, s[0:1]
  v_cndmask_b32 v50, 0, v50, s[0:1]
  v_cndmask_b32 v51, 0, v51, s[0:1]
  v_cndmask_b32 v52, 0, v52, s[0:1]
  v_cndmask_b32 v53, 0, v53, s[0:1]
  s_mov_b32 s1, 0
  v_cndmask_b32 v54, 0, v54, s[0:1]
  v_cndmask_b32 v55, 0, v55, s[0:1]
  s_branch .Lfold
.Lident:
  v_bfe_u32 v56, v0, 1, 4
  v_min_u32 v56, 13, v56
  v_cndmask_b32 v56, 63, v56, s[0:1]
  v_lshlrev_b32 v60, 2, v57
  v_sub_u32 v56, v56, v60
  v_and_b32 v60, 1, v0
  v_lshlrev_b32 v60, 4, v60
  v_mov_b32 v61, 0x3f80
  v_lshlrev_b32 v60, v60, v61
  v_cmp_eq_u32 vcc, 0, v56
  s_nop 1
  v_cndmask_b32 v48, 0, v60, vcc
  v_cmp_eq_u32 vcc, 1, v56
  s_nop 1
  v_cndmask_b32 v49, 0, v60, vcc
  v_cmp_eq_u32 vcc, 2, v56
  s_nop 1
  v_cndmask_b32 v50, 0, v60, vcc
  v_cmp_eq_u32 vcc, 3, v56
  s_nop 1
  v_cndmask_b32 v51, 0, v60, vcc
  v_cmp_eq_u32 vcc, 8, v56
  s_nop 1
  v_cndmask_b32 v52, 0, v60, vcc
  v_cmp_eq_u32 vcc, 9, v56
  s_nop 1
  v_cndmask_b32 v53, 0, v60, vcc
  v_cmp_eq_u32 vcc, 10, v56
  s_nop 1
  v_cndmask_b32 v54, 0, v60, vcc
  v_cmp_eq_u32 vcc, 11, v56
  s_nop 1
  v_cndmask_b32 v55, 0, v60, vcc
.Lfold:
  s_mov_b32 s4, s46
  s_and_b32 s5, s47, 0xffff
  s_mov_b32 s6, 21581824
  s_mov_b32 s7, 0x20000
  s_lshr_b32 s57, s49, 1
  s_mul_i32 s0, s54, 3
  s_add_u32 s0, s0, s57
  s_mul_i32 s0, s0, 14
  s_add_u32 s0, s0, s53
  s_lshl_b32 s0, s0, 11
  s_add_u32 s0, s0, 3067904
  s_lshl2_add_u32 s57, s54, s57
  s_lshl4_add_u32 s9, s57, s53
  s_lshl_b32 s1, s9, 9
  s_add_u32 s1, s1, 1363968
  s_lshl_b32 s9, s9, 7
  s_add_u32 s9, s9, 12288
  s_lshl_b32 s3, s53, 8
  s_add_u32 s3, s3, 1306624
  v_lshlrev_b32 v62, 2, v3
  v_lshlrev_b32 v61, 5, v57
  v_add_u32 v61, 160768, v61
  v_mov_b32 v60, 161024
  s_waitcnt vmcnt(0)
  ds_read_b128 v[80:83], v58
  ds_read_b128 v[84:87], v59
  ds_read_b128 v[160:163], v58 offset:2048
  ds_read_b128 v[164:167], v59 offset:2048
  s_waitcnt lgkmcnt(0)
  v_mfma_f32_32x32x16_bf16 v[64:79], v[80:83], v[48:51], 0
  v_mfma_f32_32x32x16_bf16 v[128:143], v[160:163], v[48:51], 0
  v_mfma_f32_32x32x16_bf16 v[64:79], v[84:87], v[52:55], v[64:79]
  v_mfma_f32_32x32x16_bf16 v[128:143], v[164:167], v[52:55], v[128:143]
  s_mul_i32 s50, s50, 1664525
  s_add_u32 s50, s50, 1013904223
  v_mov_b32 v2, s50
  s_cmp_lg_u32 s49, 3
  s_cbranch_scc1 .Lsbwait
  v_bfe_u32 v63, v0, 4, 1
  v_lshl_add_u32 v63, v57, 3, v63
  v_mul_u32_u24 v63, 3584, v63
  v_and_b32 v46, 0xf0, v1
  v_add_u32 v63, v63, v46
  v_lshrrev_b32 v46, 1, v46
  v_lshl_add_u32 v46, v57, 7, v46
  v_add_u32 v46, 160768, v46
  v_cvt_f32_u32 v47, v57
  s_add_u32 s57, s3, 7168
  s_add_u32 s58, s3, 14336
  s_add_u32 s8, s3, 21504
  s_sleep 16
  s_movk_i32 s59, 0x7fff
.Lgpoll_1:
  buffer_load_dwordx4 v[16:19], v63, s[4:7], s3 offen sc1
  buffer_load_dwordx4 v[20:23], v63, s[4:7], s57 offen sc1
  buffer_load_dwordx4 v[24:27], v63, s[4:7], s58 offen sc1
  buffer_load_dwordx4 v[28:31], v63, s[4:7], s8 offen sc1
  s_waitcnt vmcnt(3)
  v_cmpx_eq_u32 vcc, s50, v17
  v_cmpx_eq_u32 vcc, s50, v19
  s_waitcnt vmcnt(2)
  v_cmpx_eq_u32 vcc, s50, v21
  v_cmpx_eq_u32 vcc, s50, v23
  s_waitcnt vmcnt(1)
  v_cmpx_eq_u32 vcc, s50, v25
  v_cmpx_eq_u32 vcc, s50, v27
  s_waitcnt vmcnt(0)
  v_cmpx_eq_u32 vcc, s50, v29
  v_cmpx_eq_u32 vcc, s50, v31
  s_cmp_eq_u64 exec, -1
  s_mov_b64 exec, -1
  s_cbranch_scc1 .Lgdone_2
  s_sleep 1
  s_sub_u32 s59, s59, 1
  s_cbranch_scc0 .Lgpoll_1
.Lgdone_2:
  v_add_f32 v16, v16, v20
  v_add_f32 v18, v18, v22
  v_add_f32 v24, v24, v28
  v_add_f32 v26, v26, v30
  v_add_f32 v17, v16, v24
  v_add_f32 v19, v18, v26
  v_add_f32 v16, v16, v24
  v_add_f32 v18, v18, v26
  v_mov_b32 v20, 1
  v_permlane16_swap_b32 v16, v17
  v_permlane16_swap_b32 v18, v19
  v_add_f32 v16, v16, v17
  v_add_f32 v18, v18, v19
  v_cvt_pk_bf16_f32 v16, v16, v18
  v_and_b32 v17, 0xffff0000, v16
  v_lshlrev_b32 v16, 16, v16
  v_add_f32 v16, v16, v47
  v_add_f32 v17, v17, v47
  v_cvt_pk_bf16_f32 v16, v16, v17
  v_and_b32 v17, 0xffff0000, v16
  v_lshlrev_b32 v16, 16, v16
  ds_write_b64 v46, v[16:17]
  ds_write_b32 v60, v20
.Lsbwait:
  s_mov_b32 s59, 0x40000
  s_mov_b32 s58, 0x3f803f80
.Lsbw:
  ds_read_b32 v20, v60
  ds_read_b128 v[144:147], v61 offset:128
  ds_read_b128 v[148:151], v61 offset:144
  ds_read_b128 v[152:155], v61 offset:192
  ds_read_b128 v[156:159], v61 offset:208
  s_sub_u32 s59, s59, 1
  s_cselect_b32 s57, 0, 1
  s_waitcnt lgkmcnt(4)
  v_cmp_gt_u32 vcc, s57, v20
  s_cbranch_vccnz .Lsbw
  s_waitcnt lgkmcnt(2)
  v_pk_mul_f32 v[168:169], v[64:65], v[144:145]
  v_pk_mul_f32 v[170:171], v[66:67], v[146:147]
  v_pk_mul_f32 v[172:173], v[68:69], v[148:149]
  v_pk_mul_f32 v[174:175], v[70:71], v[150:151]
  v_cvt_pk_bf16_f32 v160, v168, v169
  v_cvt_pk_bf16_f32 v161, v170, v171
  v_cvt_pk_bf16_f32 v162, v172, v173
  v_cvt_pk_bf16_f32 v163, v174, v175
  buffer_store_dwordx4 v[160:163], v1, s[4:7], s0 offen sc1
  s_waitcnt lgkmcnt(0)
  v_pk_mul_f32 v[176:177], v[72:73], v[152:153]
  v_pk_mul_f32 v[178:179], v[74:75], v[154:155]
  v_pk_mul_f32 v[180:181], v[76:77], v[156:157]
  v_pk_mul_f32 v[182:183], v[78:79], v[158:159]
  v_cvt_pk_bf16_f32 v164, v176, v177
  v_cvt_pk_bf16_f32 v165, v178, v179
  v_cvt_pk_bf16_f32 v166, v180, v181
  v_cvt_pk_bf16_f32 v167, v182, v183
  buffer_store_dwordx4 v[164:167], v1, s[4:7], s0 offen offset:1024 sc1
  ds_read_b128 v[184:187], v61
  ds_read_b128 v[188:191], v61 offset:16
  ds_read_b128 v[192:195], v61 offset:64
  ds_read_b128 v[196:199], v61 offset:80
  v_mov_b32 v8, 0
  v_dot2c_f32_bf16 v8, s58, v160
  v_dot2c_f32_bf16 v8, s58, v161
  v_dot2c_f32_bf16 v8, s58, v162
  v_dot2c_f32_bf16 v8, s58, v163
  v_dot2c_f32_bf16 v8, s58, v164
  v_dot2c_f32_bf16 v8, s58, v165
  v_dot2c_f32_bf16 v8, s58, v166
  v_dot2c_f32_bf16 v8, s58, v167
  s_nop 2
  v_mov_b32 v9, v8
  s_nop 1
  v_permlane32_swap_b32 v8, v9
  v_add_f32 v8, v8, v9
  s_waitcnt lgkmcnt(0)
  v_pk_mul_f32 v[10:11], v[64:65], v[184:185]
  v_pk_fma_f32 v[10:11], v[66:67], v[186:187], v[10:11]
  v_pk_fma_f32 v[10:11], v[68:69], v[188:189], v[10:11]
  v_pk_fma_f32 v[10:11], v[70:71], v[190:191], v[10:11]
  v_pk_fma_f32 v[10:11], v[72:73], v[192:193], v[10:11]
  v_pk_fma_f32 v[10:11], v[74:75], v[194:195], v[10:11]
  v_pk_fma_f32 v[10:11], v[76:77], v[196:197], v[10:11]
  v_pk_fma_f32 v[10:11], v[78:79], v[198:199], v[10:11]
  v_add_f32 v10, v10, v11
  v_mov_b32 v11, v10
  s_nop 1
  v_permlane32_swap_b32 v10, v11
  v_add_f32 v10, v10, v11
  v_mov_b32 v9, v2
  v_mov_b32 v11, v2
  buffer_store_dwordx4 v[8:11], v62, s[4:7], s1 offen sc1
  s_cmp_ge_u32 s49, 2
  s_cbranch_scc1 .Lvflag
  s_add_u32 s0, s0, 57344
  s_add_u32 s1, s1, 16384
  v_pk_mul_f32 v[168:169], v[128:129], v[144:145]
  v_pk_mul_f32 v[170:171], v[130:131], v[146:147]
  v_pk_mul_f32 v[172:173], v[132:133], v[148:149]
  v_pk_mul_f32 v[174:175], v[134:135], v[150:151]
  v_cvt_pk_bf16_f32 v160, v168, v169
  v_cvt_pk_bf16_f32 v161, v170, v171
  v_cvt_pk_bf16_f32 v162, v172, v173
  v_cvt_pk_bf16_f32 v163, v174, v175
  buffer_store_dwordx4 v[160:163], v1, s[4:7], s0 offen sc1
  v_pk_mul_f32 v[176:177], v[136:137], v[152:153]
  v_pk_mul_f32 v[178:179], v[138:139], v[154:155]
  v_pk_mul_f32 v[180:181], v[140:141], v[156:157]
  v_pk_mul_f32 v[182:183], v[142:143], v[158:159]
  v_cvt_pk_bf16_f32 v164, v176, v177
  v_cvt_pk_bf16_f32 v165, v178, v179
  v_cvt_pk_bf16_f32 v166, v180, v181
  v_cvt_pk_bf16_f32 v167, v182, v183
  buffer_store_dwordx4 v[164:167], v1, s[4:7], s0 offen offset:1024 sc1
  v_mov_b32 v12, 0
  v_dot2c_f32_bf16 v12, s58, v160
  v_dot2c_f32_bf16 v12, s58, v161
  v_dot2c_f32_bf16 v12, s58, v162
  v_dot2c_f32_bf16 v12, s58, v163
  v_dot2c_f32_bf16 v12, s58, v164
  v_dot2c_f32_bf16 v12, s58, v165
  v_dot2c_f32_bf16 v12, s58, v166
  v_dot2c_f32_bf16 v12, s58, v167
  s_nop 2
  v_mov_b32 v13, v12
  s_nop 1
  v_permlane32_swap_b32 v12, v13
  v_add_f32 v12, v12, v13
  s_barrier
  s_waitcnt vmcnt(3)
  buffer_store_dword v2, v3, s[4:7], s9 offen sc1
  s_waitcnt lgkmcnt(0)
  v_pk_mul_f32 v[14:15], v[128:129], v[184:185]
  v_pk_fma_f32 v[14:15], v[130:131], v[186:187], v[14:15]
  v_pk_fma_f32 v[14:15], v[132:133], v[188:189], v[14:15]
  v_pk_fma_f32 v[14:15], v[134:135], v[190:191], v[14:15]
  v_pk_fma_f32 v[14:15], v[136:137], v[192:193], v[14:15]
  v_pk_fma_f32 v[14:15], v[138:139], v[194:195], v[14:15]
  v_pk_fma_f32 v[14:15], v[140:141], v[196:197], v[14:15]
  v_pk_fma_f32 v[14:15], v[142:143], v[198:199], v[14:15]
  v_add_f32 v14, v14, v15
  v_mov_b32 v15, v14
  s_nop 1
  v_permlane32_swap_b32 v14, v15
  v_add_f32 v14, v14, v15
  v_mov_b32 v13, v2
  v_mov_b32 v15, v2
  buffer_store_dwordx4 v[12:15], v62, s[4:7], s1 offen sc1
  s_add_u32 s9, s9, 4096
  s_waitcnt vmcnt(2)
  s_branch .Lflag2
.Lvflag:
  s_barrier
  s_waitcnt vmcnt(1)
.Lflag2:
  buffer_store_dword v2, v3, s[4:7], s9 offen sc1
  s_branch .LB
.Lprep:
  s_cmp_ge_u32 s2, 98
  s_cbranch_scc1 .LB
  s_sleep 0
  buffer_load_dwordx4 v40, s[12:15], s0 offen lds
  s_add_u32 s0, s0, s8
  buffer_load_dwordx4 v40, s[12:15], s0 offen offset:1024 lds
  s_add_u32 s0, s0, s8
  buffer_load_dwordx4 v40, s[12:15], s0 offen offset:2048 lds
  s_add_u32 s0, s0, s8
  buffer_load_dwordx4 v40, s[12:15], s0 offen offset:3072 lds
  s_add_u32 s0, s0, s8
  s_add_u32 m0, m0, 4096
  s_add_u32 s0, s0, 4096
  buffer_load_dwordx4 v40, s[12:15], s0 offen lds
  s_add_u32 s0, s0, s8
  buffer_load_dwordx4 v40, s[12:15], s0 offen offset:1024 lds
  s_add_u32 s0, s0, s8
  buffer_load_dwordx4 v40, s[12:15], s0 offen offset:2048 lds
  s_add_u32 s0, s0, s8
  buffer_load_dwordx4 v40, s[12:15], s0 offen offset:3072 lds
  s_add_u32 s0, s0, s8
  s_branch .LB
.Lrest:
  v_lshrrev_b32 v7, 3, v0
  v_mul_u32_u24 v6, 28, v7
  v_mul_u32_u24 v5, 75264, v7
  v_and_b32 v10, 0x70, v1
  v_or_b32 v5, v5, v10
  s_mul_i32 s59, s49, 224
  s_mov_b32 s14, 2408448
  s_mov_b32 s18, 896
  s_waitcnt lgkmcnt(0)
  s_sub_u32 s32, s2, 224
  s_min_u32 s32, s32, 27
  s_mul_i32 s58, s49, 602112
  s_lshl_b32 s57, s32, 7
  s_add_u32 s58, s58, s57
  s_add_u32 s58, s58, 1792
  s_load_dword s50, s[46:47], 0
  s_mov_b32 s12, s42
  s_and_b32 s13, s43, 0xffff
  s_mov_b32 s16, s40
  s_and_b32 s17, s41, 0xffff
  s_mov_b32 s4, s46
  s_and_b32 s5, s47, 0xffff
  s_mov_b32 s6, 21581824
  s_mov_b32 s7, 0x20000
  s_sleep 4
  buffer_load_dwordx4 v[8:11], v6, s[16:19], s59 offen
  buffer_load_dwordx3 v[12:14], v6, s[16:19], s59 offen offset:16
  buffer_load_dwordx4 v[128:131], v5, s[12:15], s58 offen
  s_add_u32 s58, s58, 5376
  buffer_load_dwordx4 v[132:135], v5, s[12:15], s58 offen
  s_add_u32 s58, s58, 5376
  buffer_load_dwordx4 v[136:139], v5, s[12:15], s58 offen
  s_add_u32 s58, s58, 5376
  buffer_load_dwordx4 v[140:143], v5, s[12:15], s58 offen
  s_add_u32 s58, s58, 5376
  buffer_load_dwordx4 v[144:147], v5, s[12:15], s58 offen
  s_add_u32 s58, s58, 5376
  buffer_load_dwordx4 v[148:151], v5, s[12:15], s58 offen
  s_add_u32 s58, s58, 5376
  buffer_load_dwordx4 v[152:155], v5, s[12:15], s58 offen
  s_add_u32 s58, s58, 5376
  buffer_load_dwordx4 v[156:159], v5, s[12:15], s58 offen
  s_add_u32 s58, s58, 5376
  buffer_load_dwordx4 v[160:163], v5, s[12:15], s58 offen
  s_add_u32 s58, s58, 5376
  buffer_load_dwordx4 v[164:167], v5, s[12:15], s58 offen
  s_add_u32 s58, s58, 5376
  buffer_load_dwordx4 v[168:171], v5, s[12:15], s58 offen
  s_add_u32 s58, s58, 5376
  buffer_load_dwordx4 v[172:175], v5, s[12:15], s58 offen
  s_add_u32 s58, s58, 5376
  buffer_load_dwordx4 v[176:179], v5, s[12:15], s58 offen
  s_add_u32 s58, s58, 5376
  buffer_load_dwordx4 v[180:183], v5, s[12:15], s58 offen
  s_waitcnt lgkmcnt(0)
  s_barrier
  s_mul_i32 s50, s50, 1664525
  s_add_u32 s50, s50, 1013904223
  v_mov_b32 v2, s50
  s_waitcnt vmcnt(14)
  v_lshlrev_b32 v44, 16, v8
  v_and_b32 v45, 0xffff0000, v8
  v_lshlrev_b32 v46, 16, v9
  v_and_b32 v47, 0xffff0000, v9
  v_lshlrev_b32 v48, 16, v10
  v_and_b32 v49, 0xffff0000, v10
  v_lshlrev_b32 v50, 16, v11
  v_and_b32 v51, 0xffff0000, v11
  v_lshlrev_b32 v52, 16, v12
  v_and_b32 v53, 0xffff0000, v12
  v_lshlrev_b32 v54, 16, v13
  v_and_b32 v55, 0xffff0000, v13
  v_lshlrev_b32 v56, 16, v14
  v_and_b32 v57, 0xffff0000, v14
  v_mul_f32 v64, 0xbfb8aa3b, v44
  v_mul_f32 v66, 0xbfb8aa3b, v45
  v_mul_f32 v68, 0xbfb8aa3b, v46
  v_mul_f32 v70, 0xbfb8aa3b, v47
  v_mul_f32 v72, 0xbfb8aa3b, v48
  v_mul_f32 v74, 0xbfb8aa3b, v49
  v_mul_f32 v76, 0xbfb8aa3b, v50
  v_mul_f32 v78, 0xbfb8aa3b, v51
  v_mul_f32 v80, 0xbfb8aa3b, v52
  v_mul_f32 v82, 0xbfb8aa3b, v53
  v_mul_f32 v84, 0xbfb8aa3b, v54
  v_mul_f32 v86, 0xbfb8aa3b, v55
  v_mul_f32 v88, 0xbfb8aa3b, v56
  v_mul_f32 v90, 0xbfb8aa3b, v57
  v_exp_f32 v64, v64
  v_exp_f32 v66, v66
  v_exp_f32 v68, v68
  v_exp_f32 v70, v70
  v_exp_f32 v72, v72
  v_exp_f32 v74, v74
  v_exp_f32 v76, v76
  v_exp_f32 v78, v78
  v_exp_f32 v80, v80
  v_exp_f32 v82, v82
  v_exp_f32 v84, v84
  v_exp_f32 v86, v86
  v_exp_f32 v88, v88
  v_exp_f32 v90, v90
  v_add_f32 v64, 1.0, v64
  v_add_f32 v66, 1.0, v66
  v_add_f32 v68, 1.0, v68
  v_add_f32 v70, 1.0, v70
  v_add_f32 v72, 1.0, v72
  v_add_f32 v74, 1.0, v74
  v_add_f32 v76, 1.0, v76
  v_add_f32 v78, 1.0, v78
  v_add_f32 v80, 1.0, v80
  v_add_f32 v82, 1.0, v82
  v_add_f32 v84, 1.0, v84
  v_add_f32 v86, 1.0, v86
  v_add_f32 v88, 1.0, v88
  v_add_f32 v90, 1.0, v90
  v_rcp_f32 v64, v64
  v_rcp_f32 v66, v66
  v_rcp_f32 v68, v68
  v_rcp_f32 v70, v70
  v_rcp_f32 v72, v72
  v_rcp_f32 v74, v74
  v_rcp_f32 v76, v76
  v_rcp_f32 v78, v78
  v_rcp_f32 v80, v80
  v_rcp_f32 v82, v82
  v_rcp_f32 v84, v84
  v_rcp_f32 v86, v86
  v_rcp_f32 v88, v88
  v_rcp_f32 v90, v90
  v_mul_f32 v44, v44, v64
  v_mul_f32 v45, v45, v66
  v_mul_f32 v46, v46, v68
  v_mul_f32 v47, v47, v70
  v_mul_f32 v48, v48, v72
  v_mul_f32 v49, v49, v74
  v_mul_f32 v50, v50, v76
  v_mul_f32 v51, v51, v78
  v_mul_f32 v52, v52, v80
  v_mul_f32 v53, v53, v82
  v_mul_f32 v54, v54, v84
  v_mul_f32 v55, v55, v86
  v_mul_f32 v56, v56, v88
  v_mul_f32 v57, v57, v90
  v_cvt_pk_bf16_f32 v64, v44, 0
  v_cvt_pk_bf16_f32 v65, 0, v44
  v_cvt_pk_bf16_f32 v66, v45, 0
  v_cvt_pk_bf16_f32 v67, 0, v45
  v_cvt_pk_bf16_f32 v68, v46, 0
  v_cvt_pk_bf16_f32 v69, 0, v46
  v_cvt_pk_bf16_f32 v70, v47, 0
  v_cvt_pk_bf16_f32 v71, 0, v47
  v_cvt_pk_bf16_f32 v72, v48, 0
  v_cvt_pk_bf16_f32 v73, 0, v48
  v_cvt_pk_bf16_f32 v74, v49, 0
  v_cvt_pk_bf16_f32 v75, 0, v49
  v_cvt_pk_bf16_f32 v76, v50, 0
  v_cvt_pk_bf16_f32 v77, 0, v50
  v_cvt_pk_bf16_f32 v78, v51, 0
  v_cvt_pk_bf16_f32 v79, 0, v51
  v_cvt_pk_bf16_f32 v80, v52, 0
  v_cvt_pk_bf16_f32 v81, 0, v52
  v_cvt_pk_bf16_f32 v82, v53, 0
  v_cvt_pk_bf16_f32 v83, 0, v53
  v_cvt_pk_bf16_f32 v84, v54, 0
  v_cvt_pk_bf16_f32 v85, 0, v54
  v_cvt_pk_bf16_f32 v86, v55, 0
  v_cvt_pk_bf16_f32 v87, 0, v55
  v_cvt_pk_bf16_f32 v88, v56, 0
  v_cvt_pk_bf16_f32 v89, 0, v56
  v_cvt_pk_bf16_f32 v90, v57, 0
  v_cvt_pk_bf16_f32 v91, 0, v57
  v_mov_b32 v8, 0
  v_mov_b32 v9, 0
  v_mov_b32 v10, 0
  v_mov_b32 v11, 0
  v_mov_b32 v12, 0
  v_mov_b32 v13, 0
  v_mov_b32 v14, 0
  v_mov_b32 v15, 0
  s_waitcnt vmcnt(13)
  v_dot2c_f32_bf16 v8, v128, v64
  v_dot2c_f32_bf16 v9, v128, v65
  v_dot2c_f32_bf16 v10, v129, v64
  v_dot2c_f32_bf16 v11, v129, v65
  v_dot2c_f32_bf16 v12, v130, v64
  v_dot2c_f32_bf16 v13, v130, v65
  v_dot2c_f32_bf16 v14, v131, v64
  v_dot2c_f32_bf16 v15, v131, v65
  s_waitcnt vmcnt(12)
  v_dot2c_f32_bf16 v8, v132, v66
  v_dot2c_f32_bf16 v9, v132, v67
  v_dot2c_f32_bf16 v10, v133, v66
  v_dot2c_f32_bf16 v11, v133, v67
  v_dot2c_f32_bf16 v12, v134, v66
  v_dot2c_f32_bf16 v13, v134, v67
  v_dot2c_f32_bf16 v14, v135, v66
  v_dot2c_f32_bf16 v15, v135, v67
  s_waitcnt vmcnt(11)
  v_dot2c_f32_bf16 v8, v136, v68
  v_dot2c_f32_bf16 v9, v136, v69
  v_dot2c_f32_bf16 v10, v137, v68
  v_dot2c_f32_bf16 v11, v137, v69
  v_dot2c_f32_bf16 v12, v138, v68
  v_dot2c_f32_bf16 v13, v138, v69
  v_dot2c_f32_bf16 v14, v139, v68
  v_dot2c_f32_bf16 v15, v139, v69
  s_waitcnt vmcnt(10)
  v_dot2c_f32_bf16 v8, v140, v70
  v_dot2c_f32_bf16 v9, v140, v71
  v_dot2c_f32_bf16 v10, v141, v70
  v_dot2c_f32_bf16 v11, v141, v71
  v_dot2c_f32_bf16 v12, v142, v70
  v_dot2c_f32_bf16 v13, v142, v71
  v_dot2c_f32_bf16 v14, v143, v70
  v_dot2c_f32_bf16 v15, v143, v71
  s_waitcnt vmcnt(9)
  v_dot2c_f32_bf16 v8, v144, v72
  v_dot2c_f32_bf16 v9, v144, v73
  v_dot2c_f32_bf16 v10, v145, v72
  v_dot2c_f32_bf16 v11, v145, v73
  v_dot2c_f32_bf16 v12, v146, v72
  v_dot2c_f32_bf16 v13, v146, v73
  v_dot2c_f32_bf16 v14, v147, v72
  v_dot2c_f32_bf16 v15, v147, v73
  s_waitcnt vmcnt(8)
  v_dot2c_f32_bf16 v8, v148, v74
  v_dot2c_f32_bf16 v9, v148, v75
  v_dot2c_f32_bf16 v10, v149, v74
  v_dot2c_f32_bf16 v11, v149, v75
  v_dot2c_f32_bf16 v12, v150, v74
  v_dot2c_f32_bf16 v13, v150, v75
  v_dot2c_f32_bf16 v14, v151, v74
  v_dot2c_f32_bf16 v15, v151, v75
  s_waitcnt vmcnt(7)
  v_dot2c_f32_bf16 v8, v152, v76
  v_dot2c_f32_bf16 v9, v152, v77
  v_dot2c_f32_bf16 v10, v153, v76
  v_dot2c_f32_bf16 v11, v153, v77
  v_dot2c_f32_bf16 v12, v154, v76
  v_dot2c_f32_bf16 v13, v154, v77
  v_dot2c_f32_bf16 v14, v155, v76
  v_dot2c_f32_bf16 v15, v155, v77
  s_waitcnt vmcnt(6)
  v_dot2c_f32_bf16 v8, v156, v78
  v_dot2c_f32_bf16 v9, v156, v79
  v_dot2c_f32_bf16 v10, v157, v78
  v_dot2c_f32_bf16 v11, v157, v79
  v_dot2c_f32_bf16 v12, v158, v78
  v_dot2c_f32_bf16 v13, v158, v79
  v_dot2c_f32_bf16 v14, v159, v78
  v_dot2c_f32_bf16 v15, v159, v79
  s_waitcnt vmcnt(5)
  v_dot2c_f32_bf16 v8, v160, v80
  v_dot2c_f32_bf16 v9, v160, v81
  v_dot2c_f32_bf16 v10, v161, v80
  v_dot2c_f32_bf16 v11, v161, v81
  v_dot2c_f32_bf16 v12, v162, v80
  v_dot2c_f32_bf16 v13, v162, v81
  v_dot2c_f32_bf16 v14, v163, v80
  v_dot2c_f32_bf16 v15, v163, v81
  s_waitcnt vmcnt(4)
  v_dot2c_f32_bf16 v8, v164, v82
  v_dot2c_f32_bf16 v9, v164, v83
  v_dot2c_f32_bf16 v10, v165, v82
  v_dot2c_f32_bf16 v11, v165, v83
  v_dot2c_f32_bf16 v12, v166, v82
  v_dot2c_f32_bf16 v13, v166, v83
  v_dot2c_f32_bf16 v14, v167, v82
  v_dot2c_f32_bf16 v15, v167, v83
  s_waitcnt vmcnt(3)
  v_dot2c_f32_bf16 v8, v168, v84
  v_dot2c_f32_bf16 v9, v168, v85
  v_dot2c_f32_bf16 v10, v169, v84
  v_dot2c_f32_bf16 v11, v169, v85
  v_dot2c_f32_bf16 v12, v170, v84
  v_dot2c_f32_bf16 v13, v170, v85
  v_dot2c_f32_bf16 v14, v171, v84
  v_dot2c_f32_bf16 v15, v171, v85
  s_waitcnt vmcnt(2)
  v_dot2c_f32_bf16 v8, v172, v86
  v_dot2c_f32_bf16 v9, v172, v87
  v_dot2c_f32_bf16 v10, v173, v86
  v_dot2c_f32_bf16 v11, v173, v87
  v_dot2c_f32_bf16 v12, v174, v86
  v_dot2c_f32_bf16 v13, v174, v87
  v_dot2c_f32_bf16 v14, v175, v86
  v_dot2c_f32_bf16 v15, v175, v87
  s_waitcnt vmcnt(1)
  v_dot2c_f32_bf16 v8, v176, v88
  v_dot2c_f32_bf16 v9, v176, v89
  v_dot2c_f32_bf16 v10, v177, v88
  v_dot2c_f32_bf16 v11, v177, v89
  v_dot2c_f32_bf16 v12, v178, v88
  v_dot2c_f32_bf16 v13, v178, v89
  v_dot2c_f32_bf16 v14, v179, v88
  v_dot2c_f32_bf16 v15, v179, v89
  s_waitcnt vmcnt(0)
  v_dot2c_f32_bf16 v8, v180, v90
  v_dot2c_f32_bf16 v9, v180, v91
  v_dot2c_f32_bf16 v10, v181, v90
  v_dot2c_f32_bf16 v11, v181, v91
  v_dot2c_f32_bf16 v12, v182, v90
  v_dot2c_f32_bf16 v13, v182, v91
  v_dot2c_f32_bf16 v14, v183, v90
  v_dot2c_f32_bf16 v15, v183, v91
  v_permlane32_swap_b32 v8, v12
  v_permlane32_swap_b32 v9, v13
  v_permlane32_swap_b32 v10, v14
  v_permlane32_swap_b32 v11, v15
  v_add_f32 v8, v8, v12
  v_add_f32 v9, v9, v13
  v_add_f32 v10, v10, v14
  v_add_f32 v11, v11, v15
  s_nop 0
  v_permlane16_swap_b32 v8, v10
  v_permlane16_swap_b32 v9, v11
  v_add_f32 v8, v8, v10
  v_add_f32 v9, v9, v11
  s_nop 0
  v_add_f32_dpp v8, v8, v8 row_ror:8 row_mask:0xf bank_mask:0xf
  v_add_f32_dpp v9, v9, v9 row_ror:8 row_mask:0xf bank_mask:0xf
  v_and_b32 v16, 7, v0
  v_lshlrev_b32 v16, 5, v16
  v_lshrrev_b32 v17, 4, v0
  v_lshl_or_b32 v16, v17, 3, v16
  s_lshl_b32 s57, s49, 8
  s_add_u32 s57, s57, 114688
  v_add_u32 v16, s57, v16
  ds_write_b64 v16, v[8:9]
  v_mov_b32 v52, 161024
  v_mov_b32 v53, 1
  s_mov_b64 exec, 1
  ds_add_u32 v52, v53
  s_mov_b64 exec, -1
  s_movk_i32 s59, 0x7fff
.Lbs_3:
  ds_read_b32 v53, v52
  s_sub_u32 s59, s59, 1
  s_cselect_b32 s57, 0, 4
  s_waitcnt lgkmcnt(0)
  v_cmp_gt_u32 vcc, s57, v53
  s_cbranch_vccnz .Lbs_3
  v_lshlrev_b32 v16, 2, v0
  v_add_u32 v16, 114688, v16
  ds_read2st64_b32 v[8:9], v16 offset0:0 offset1:1
  ds_read2st64_b32 v[10:11], v16 offset0:2 offset1:3
  s_cmp_lt_u32 s2, 252
  s_cselect_b32 s57, 0, 2146435072
  s_cmp_eq_u32 s49, 0
  s_cselect_b32 s57, s57, 2146435072
  v_lshl_or_b32 v16, v0, 3, s57
  s_sub_u32 s57, s32, 14
  s_cmp_lt_u32 s57, 7
  s_cselect_b32 s58, 1.0, 0
  s_lshl_b32 s57, s32, 9
  s_add_u32 s57, s57, 1290240
  s_waitcnt lgkmcnt(0)
  v_add_f32 v8, v8, v9
  v_add_f32 v10, v10, v11
  v_add_f32 v8, v8, v10
  v_cvt_pk_bf16_f32 v8, v8, v8
  v_and_b32 v8, 0xffff0000, v8
  v_add_f32 v8, s58, v8
  v_cvt_pk_bf16_f32 v8, v8, v8
  v_and_b32 v8, 0xffff0000, v8
  v_mov_b32 v9, v2
  buffer_store_dwordx2 v[8:9], v16, s[4:7], s57 offen sc1
  s_sleep 16
.LB:
  s_min_u32 s57, s56, 27
  s_mul_i32 s58, s57, 37
  s_lshr_b32 s58, s58, 8
  s_mul_i32 s53, s58, 7
  s_sub_u32 s53, s57, s53
  s_lshr_b32 s54, s55, 1
  s_lshl2_add_u32 s54, s54, s58
  s_and_b32 s55, s55, 1
  s_mul_i32 s0, s52, 86016
  s_add_u32 s0, s0, 3067904
  s_lshl_b32 s3, s49, 10
  s_add_u32 m0, s3, 114688
  s_add_u32 s15, s3, 28672
  s_add_u32 s0, s0, s3
  s_lshl_b32 s8, s23, 12
  v_add_u32 v15, 28672, v1
  v_add_u32 v16, 57344, v1
  s_lshl3_add_u32 s16, s52, s49
  s_lshl_b32 s16, s16, 12
  s_add_u32 s16, s16, 1363968
  s_bitcmp1_b32 s49, 0
  s_cselect_b32 s17, 0, 1.0
  s_cselect_b32 s57, 1024, 0
  s_cselect_b32 s58, 8, 0
  s_sub_u32 s18, s16, s57
  s_lshr_b32 s57, s49, 1
  s_lshl_b32 s57, s57, 10
  s_add_u32 s57, s57, s58
  s_add_u32 s57, s57, 155648
  v_add_u32 v17, s57, v1
  v_and_b32 v12, 15, v0
  v_min_u32 v12, 13, v12
  v_lshlrev_b32 v14, 7, v12
  v_and_b32 v13, 16, v0
  v_lshl_add_u32 v12, v13, 7, v14
  s_lshl_b32 s1, s52, 13
  s_add_u32 s1, s1, 12288
  s_add_u32 s19, s1, 4096
  s_cmp_eq_u32 s49, 3
  s_cbranch_scc1 .Lqf
  v_and_b32 v14, 60, v3
  s_lshl_b32 s19, s53, 6
  s_add_u32 s19, s19, 8192
.Lqf:
  s_add_u32 s9, s8, 4096
  s_cmp_eq_u32 s9, 28672
  s_cselect_b32 s9, 0, s9
  s_add_u32 s10, s9, 4096
  s_cmp_eq_u32 s10, 28672
  s_cselect_b32 s10, 0, s10
  s_add_u32 s11, s10, 4096
  s_cmp_eq_u32 s11, 28672
  s_cselect_b32 s11, 0, s11
  s_add_u32 s12, s11, 4096
  s_cmp_eq_u32 s12, 28672
  s_cselect_b32 s12, 0, s12
  s_add_u32 s13, s12, 4096
  s_cmp_eq_u32 s13, 28672
  s_cselect_b32 s13, 0, s13
  s_add_u32 s14, s13, 4096
  s_cmp_eq_u32 s14, 28672
  s_cselect_b32 s14, 0, s14
  s_add_u32 s8, s8, s0
  s_add_u32 s9, s9, s0
  s_add_u32 s10, s10, s0
  s_add_u32 s11, s11, s0
  s_add_u32 s12, s12, s0
  s_add_u32 s13, s13, s0
  s_add_u32 s14, s14, s0
  s_lshl_b32 s3, s49, 14
  s_xor_b32 s57, s49, s53
  s_and_b32 s57, s57, 3
  s_lshl2_add_u32 s0, s53, s57
  s_lshl_b32 s0, s0, 14
  s_add_u32 s0, s0, 2609152
  s_add_u32 s3, s3, 73728
  v_lshrrev_b32 v8, 2, v0
  v_bfe_u32 v9, v0, 4, 1
  v_lshrrev_b32 v10, 5, v0
  v_lshlrev_b32 v10, 1, v10
  v_lshl_or_b32 v9, v9, 2, v10
  v_bfe_u32 v10, v0, 3, 1
  v_or_b32 v9, v9, v10
  v_and_b32 v10, 3, v0
  v_xor_b32 v9, v9, v10
  v_lshlrev_b32 v9, 3, v9
  v_lshl_or_b32 v6, v8, 6, v9
  v_xor_b32 v7, 32, v6
  s_and_b32 s57, s49, 1
  s_lshl_b32 s57, s57, 5
  s_lshr_b32 s58, s49, 1
  s_lshl_b32 s58, s58, 13
  s_or_b32 s57, s57, s58
  v_xor_b32 v5, s57, v6
  s_lshl4_add_u32 s41, s52, s51
  s_lshl2_add_u32 s40, s41, s49
  s_lshl_b32 s40, s40, 10
  s_add_u32 s40, s40, 4444160
  s_lshl_b32 s41, s41, 7
  s_add_u32 s41, s41, 143360
  v_and_b32 v96, 31, v0
  v_bfe_u32 v97, v0, 1, 3
  v_lshrrev_b32 v98, 5, v0
  v_xor_b32 v97, v97, v98
  v_lshlrev_b32 v97, 4, v97
  v_lshl_or_b32 v96, v96, 7, v97
  s_mul_i32 s57, s49, 28672
  v_add_u32 v96, s57, v96
  v_xor_b32 v97, 32, v96
  v_xor_b32 v98, 64, v96
  v_xor_b32 v99, 96, v96
  v_bfe_u32 v8, v0, 2, 1
  v_bfe_u32 v9, v0, 3, 1
  v_lshlrev_b32 v9, 1, v9
  v_lshl_or_b32 v8, v8, 2, v9
  v_bfe_u32 v9, v0, 1, 1
  v_or_b32 v8, v8, v9
  v_lshrrev_b32 v9, 5, v0
  v_xor_b32 v8, v8, v9
  v_lshlrev_b32 v8, 3, v8
  v_lshl_or_b32 v8, v3, 4, v8
  s_lshl_b32 s57, s49, 11
  v_add_u32 v116, s57, v8
  v_xor_b32 v117, 16, v116
  v_xor_b32 v118, 32, v116
  v_xor_b32 v119, 48, v116
  s_lshl_b32 s57, s49, 9
  s_add_u32 s57, s57, 158720
  v_lshl_add_u32 v114, v3, 2, s57
  v_add_u32 v120, 114688, v1
  v_bfe_u32 v46, v0, 4, 1
  v_lshlrev_b32 v46, 14, v46
  v_lshrrev_b32 v47, 5, v0
  v_lshl_or_b32 v46, v47, 8, v46
  v_and_b32 v47, 15, v0
  v_lshl_or_b32 v46, v47, 4, v46
  s_lshl2_add_u32 s42, s51, s49
  s_lshl_b32 s42, s42, 5
  s_add_u32 s42, s42, s52
  s_lshl_b32 s42, s42, 10
  s_add_u32 s42, s42, 5492736
  v_mov_b32 v121, v46
  v_mov_b32 v252, 2139193217
  v_mov_b32 v253, 2139193217
  v_mov_b32 v254, 2139193217
  v_mov_b32 v255, 2139193217
  s_cmp_eq_u32 s49, 3
  s_cselect_b32 s57, 0, 2146435072
  v_or_b32 v115, s57, v3
  s_cmp_ge_u32 s56, 14
  s_cbranch_scc1 .Lnotp
  v_cmp_eq_u32 vcc, 0, v0
  s_waitcnt vmcnt(0)
  s_nop 0
  v_cndmask_b32 v8, v4, v115, vcc
  buffer_store_dword v2, v8, s[4:7], s43 offen sc1
  s_cmp_ge_u32 s2, 98
  s_cbranch_scc1 .Lpidle
  v_mov_b32 v52, 161024
  v_mov_b32 v53, 1
  s_mov_b64 exec, 1
  ds_add_u32 v52, v53
  s_mov_b64 exec, -1
  s_movk_i32 s59, 0x7fff
.Lbs_4:
  ds_read_b32 v53, v52
  s_sub_u32 s59, s59, 1
  s_cselect_b32 s57, 0, 4
  s_waitcnt lgkmcnt(0)
  v_cmp_gt_u32 vcc, s57, v53
  s_cbranch_vccnz .Lbs_4
  ds_read_b64_tr_b16 v[64:65], v48
  ds_read_b64_tr_b16 v[66:67], v49
  ds_read_b64_tr_b16 v[80:81], v50
  ds_read_b64_tr_b16 v[82:83], v51
  ds_read_b64_tr_b16 v[68:69], v48 offset:4096
  ds_read_b64_tr_b16 v[70:71], v49 offset:4096
  ds_read_b64_tr_b16 v[84:85], v50 offset:4096
  ds_read_b64_tr_b16 v[86:87], v51 offset:4096
  ds_read_b64_tr_b16 v[72:73], v48 offset:8192
  ds_read_b64_tr_b16 v[74:75], v49 offset:8192
  ds_read_b64_tr_b16 v[88:89], v50 offset:8192
  ds_read_b64_tr_b16 v[90:91], v51 offset:8192
  ds_read_b64_tr_b16 v[76:77], v48 offset:12288
  ds_read_b64_tr_b16 v[78:79], v49 offset:12288
  ds_read_b64_tr_b16 v[92:93], v50 offset:12288
  ds_read_b64_tr_b16 v[94:95], v51 offset:12288
  s_waitcnt lgkmcnt(0)
.Lpidle:
  s_sleep 6
.Lnotp:
  s_cmp_lg_u32 s49, 3
  s_cbranch_scc1 .Lwgo
  s_sleep 0
  s_movk_i32 s59, 0x7fff
.Lpoll_5:
  buffer_load_dword v8, v12, s[4:7], s1 offen sc1
  s_waitcnt vmcnt(0)
  v_cmp_ne_u32 vcc, s50, v8
  s_cbranch_vccz .Ldone_6
  s_sleep 1
  s_sub_u32 s59, s59, 1
  s_cbranch_scc0 .Lpoll_5
.Ldone_6:
.Lwgo:
  s_barrier
  buffer_load_dwordx4 v1, s[4:7], s8 offen sc1 lds
  s_add_u32 m0, m0, 4096
  s_nop 0
  buffer_load_dwordx4 v1, s[4:7], s9 offen sc1 lds
  s_add_u32 m0, m0, 4096
  s_nop 0
  buffer_load_dwordx4 v1, s[4:7], s10 offen sc1 lds
  s_add_u32 m0, m0, 4096
  s_nop 0
  buffer_load_dwordx4 v1, s[4:7], s11 offen sc1 lds
  s_add_u32 m0, m0, 4096
  s_nop 0
  buffer_load_dwordx4 v1, s[4:7], s12 offen sc1 lds
  s_add_u32 m0, m0, 4096
  s_nop 0
  buffer_load_dwordx4 v1, s[4:7], s13 offen sc1 lds
  s_add_u32 m0, m0, 4096
  s_nop 0
  buffer_load_dwordx4 v1, s[4:7], s14 offen sc1 lds
  s_add_u32 m0, m0, 4096
  ds_read_b128 v[128:131], v96
  ds_read_b128 v[132:135], v97
  ds_read_b128 v[136:139], v98
  ds_read_b128 v[140:143], v99
  ds_read_b128 v[144:147], v96 offset:4096
  ds_read_b128 v[148:151], v97 offset:4096
  ds_read_b128 v[152:155], v98 offset:4096
  ds_read_b128 v[156:159], v99 offset:4096
  ds_read_b128 v[160:163], v96 offset:8192
  ds_read_b128 v[164:167], v97 offset:8192
  ds_read_b128 v[168:171], v98 offset:8192
  ds_read_b128 v[172:175], v99 offset:8192
  ds_read_b128 v[176:179], v96 offset:12288
  ds_read_b128 v[180:183], v97 offset:12288
  ds_read_b128 v[184:187], v98 offset:12288
  ds_read_b128 v[188:191], v99 offset:12288
  ds_read_b128 v[192:195], v96 offset:16384
  ds_read_b128 v[196:199], v97 offset:16384
  ds_read_b128 v[200:203], v98 offset:16384
  ds_read_b128 v[204:207], v99 offset:16384
  ds_read_b128 v[208:211], v96 offset:20480
  ds_read_b128 v[212:215], v97 offset:20480
  ds_read_b128 v[216:219], v98 offset:20480
  ds_read_b128 v[220:223], v99 offset:20480
  ds_read_b128 v[224:227], v96 offset:24576
  ds_read_b128 v[228:231], v97 offset:24576
  ds_read_b128 v[232:235], v98 offset:24576
  ds_read_b128 v[236:239], v99 offset:24576
  buffer_load_dword v8, v14, s[4:7], s19 offen sc1
  buffer_load_dwordx4 v[96:99], v1, s[4:7], s16 offen sc1
  buffer_load_dwordx4 v[100:103], v1, s[4:7], s16 offen offset:1024 sc1
  buffer_load_dwordx4 v[104:107], v1, s[4:7], s16 offen offset:2048 sc1
  buffer_load_dwordx4 v[108:111], v1, s[4:7], s18 offen offset:3072 sc1
  buffer_load_dwordx4 v15, s[4:7], s8 offen sc1 lds
  s_add_u32 m0, m0, 4096
  s_nop 0
  buffer_load_dwordx4 v15, s[4:7], s9 offen sc1 lds
  s_add_u32 m0, m0, 4096
  s_nop 0
  buffer_load_dwordx4 v15, s[4:7], s10 offen sc1 lds
  s_add_u32 m0, m0, 4096
  s_mov_b32 m0, s15
  s_waitcnt vmcnt(12)
  s_waitcnt lgkmcnt(0)
  s_barrier
  buffer_load_dwordx4 v15, s[4:7], s11 offen sc1 lds
  s_add_u32 m0, m0, 4096
  s_nop 0
  buffer_load_dwordx4 v15, s[4:7], s12 offen sc1 lds
  s_add_u32 m0, m0, 4096
  s_nop 0
  buffer_load_dwordx4 v15, s[4:7], s13 offen sc1 lds
  s_add_u32 m0, m0, 4096
  s_nop 0
  buffer_load_dwordx4 v15, s[4:7], s14 offen sc1 lds
  s_add_u32 m0, m0, 4096
  s_waitcnt vmcnt(11)
  v_cmp_ne_u32 vcc, s50, v8
  s_cbranch_vccz .Ldone_9
  s_movk_i32 s59, 0x7fff
.Lpoll_8:
  buffer_load_dword v8, v14, s[4:7], s19 offen sc1
  s_waitcnt vmcnt(0)
  v_cmp_ne_u32 vcc, s50, v8
  s_cbranch_vccz .Ldone_9
  s_sleep 1
  s_sub_u32 s59, s59, 1
  s_cbranch_scc0 .Lpoll_8
.Ldone_9:
  s_barrier
  buffer_load_dwordx4 v16, s[4:7], s8 offen sc1 lds
  s_add_u32 m0, m0, 4096
  s_nop 0
  buffer_load_dwordx4 v16, s[4:7], s9 offen sc1 lds
  s_add_u32 m0, m0, 4096
  s_nop 0
  buffer_load_dwordx4 v16, s[4:7], s10 offen sc1 lds
  s_add_u32 m0, m0, 4096
  s_waitcnt vmcnt(10)
  v_cmpx_eq_u32 vcc, s50, v97
  v_cmpx_eq_u32 vcc, s50, v99
  v_cmpx_eq_u32 vcc, s50, v101
  v_cmpx_eq_u32 vcc, s50, v103
  v_cmpx_eq_u32 vcc, s50, v105
  v_cmpx_eq_u32 vcc, s50, v107
  v_cmpx_eq_u32 vcc, s50, v109
  v_cmpx_eq_u32 vcc, s50, v111
  s_cmp_eq_u64 exec, -1
  s_mov_b64 exec, -1
  s_cbranch_scc1 .Legok_11
  s_movk_i32 s59, 0x7fff
.Lgpoll_12:
  buffer_load_dwordx4 v[96:99], v1, s[4:7], s16 offen sc1
  buffer_load_dwordx4 v[100:103], v1, s[4:7], s16 offen offset:1024 sc1
  buffer_load_dwordx4 v[104:107], v1, s[4:7], s16 offen offset:2048 sc1
  buffer_load_dwordx4 v[108:111], v1, s[4:7], s18 offen offset:3072 sc1
  s_waitcnt vmcnt(3)
  v_cmpx_eq_u32 vcc, s50, v97
  v_cmpx_eq_u32 vcc, s50, v99
  s_waitcnt vmcnt(2)
  v_cmpx_eq_u32 vcc, s50, v101
  v_cmpx_eq_u32 vcc, s50, v103
  s_waitcnt vmcnt(1)
  v_cmpx_eq_u32 vcc, s50, v105
  v_cmpx_eq_u32 vcc, s50, v107
  s_waitcnt vmcnt(0)
  v_cmpx_eq_u32 vcc, s50, v109
  v_cmpx_eq_u32 vcc, s50, v111
  s_cmp_eq_u64 exec, -1
  s_mov_b64 exec, -1
  s_cbranch_scc1 .Lgdone_13
  s_sleep 1
  s_sub_u32 s59, s59, 1
  s_cbranch_scc0 .Lgpoll_12
.Lgdone_13:
.Legok_11:
  v_add_f32 v96, v96, v100
  v_fmac_f32 v104, s17, v108
  v_add_f32 v96, v96, v104
  v_add_f32 v98, v98, v102
  v_fmac_f32 v106, s17, v110
  v_add_f32 v98, v98, v106
  v_cvt_pk_bf16_f32 v100, v96, v96
  v_and_b32 v100, 0xffff0000, v100
  v_sub_f32 v96, v96, v100
  v_cvt_pk_bf16_f32 v96, v100, v96
  v_cvt_pk_bf16_f32 v101, v98, v98
  v_and_b32 v101, 0xffff0000, v101
  v_sub_f32 v97, v98, v101
  v_cvt_pk_bf16_f32 v97, v101, v97
  ds_write_b64 v17, v[96:97]
  buffer_load_dwordx4 v16, s[4:7], s11 offen sc1 lds
  s_add_u32 m0, m0, 4096
  s_nop 0
  buffer_load_dwordx4 v16, s[4:7], s12 offen sc1 lds
  s_add_u32 m0, m0, 4096
  s_nop 0
  buffer_load_dwordx4 v16, s[4:7], s13 offen sc1 lds
  s_add_u32 m0, m0, 4096
  s_nop 0
  buffer_load_dwordx4 v16, s[4:7], s14 offen sc1 lds
  s_add_u32 m0, m0, 4096
  s_waitcnt vmcnt(11)
  s_waitcnt lgkmcnt(0)
  s_barrier
  ds_read_b128 v[244:247], v114
  ds_read_b128 v[240:243], v120 offset:41984
  ds_read_b128 v[40:43], v120 offset:28672
  ds_read_b128 v[44:47], v120 offset:29696
  ds_read_b128 v[48:51], v120 offset:30720
  ds_read_b128 v[52:55], v120 offset:31744
  s_and_b32 s57, s49, 2
  s_lshl_b32 s57, s57, 12
  s_sub_u32 s57, 16384, s57
  s_add_u32 s16, s16, s57
  s_add_u32 s18, s18, s57
  v_and_b32 v17, 0xfffffbff, v17
  buffer_load_dwordx4 v[96:99], v1, s[4:7], s16 offen sc1
  buffer_load_dwordx4 v[100:103], v1, s[4:7], s16 offen offset:1024 sc1
  buffer_load_dwordx4 v[104:107], v1, s[4:7], s16 offen offset:2048 sc1
  buffer_load_dwordx4 v[108:111], v1, s[4:7], s18 offen offset:3072 sc1
  s_waitcnt lgkmcnt(5)
  v_mov_b32 v112, v246
  v_mov_b32 v246, v244
  v_mov_b32 v247, v245
  s_cmp_lt_u32 s2, 98
  s_barrier
  s_cbranch_scc1 .Lvp
  s_waitcnt lgkmcnt(3)
  v_mfma_f32_32x32x16_bf16 v[24:39], v[40:43], v[128:131], 0
  ds_read_b128 v[56:59], v120 offset:32768
  buffer_store_dwordx4 v[252:255], v1, s[4:7], s40 offen sc1
  s_waitcnt lgkmcnt(3)
  v_mfma_f32_32x32x16_bf16 v[24:39], v[44:47], v[132:135], v[24:39]
  ds_read_b128 v[60:63], v120 offset:33792
  s_waitcnt lgkmcnt(3)
  v_mfma_f32_32x32x16_bf16 v[24:39], v[48:51], v[136:139], v[24:39]
  ds_read_b128 v[40:43], v120 offset:34816
  s_add_u32 m0, s3, 0
  s_add_u32 s57, s0, 0
  buffer_load_dwordx4 v1, s[4:7], s57 offen sc1 lds
  s_waitcnt lgkmcnt(3)
  v_mfma_f32_32x32x16_bf16 v[24:39], v[52:55], v[140:143], v[24:39]
  ds_read_b128 v[44:47], v120 offset:35840
  s_waitcnt lgkmcnt(3)
  v_mfma_f32_32x32x16_bf16 v[24:39], v[56:59], v[144:147], v[24:39]
  ds_read_b128 v[48:51], v120 offset:36864
  buffer_load_dwordx4 v1, s[4:7], s57 offen offset:1024 sc1 lds
  s_waitcnt lgkmcnt(3)
  v_mfma_f32_32x32x16_bf16 v[24:39], v[60:63], v[148:151], v[24:39]
  ds_read_b128 v[52:55], v120 offset:37888
  s_waitcnt lgkmcnt(3)
  v_mfma_f32_32x32x16_bf16 v[24:39], v[40:43], v[152:155], v[24:39]
  ds_read_b128 v[56:59], v120 offset:38912
  buffer_load_dwordx4 v1, s[4:7], s57 offen offset:2048 sc1 lds
  s_waitcnt lgkmcnt(3)
  v_mfma_f32_32x32x16_bf16 v[24:39], v[44:47], v[156:159], v[24:39]
  ds_read_b128 v[60:63], v120 offset:39936
  s_waitcnt lgkmcnt(3)
  v_mfma_f32_32x32x16_bf16 v[24:39], v[48:51], v[160:163], v[24:39]
  s_waitcnt vmcnt(15)
  s_barrier
  ds_read_b128 v[40:43], v1 offset:28672
  buffer_load_dwordx4 v1, s[4:7], s57 offen offset:3072 sc1 lds
  s_waitcnt lgkmcnt(3)
  v_mfma_f32_32x32x16_bf16 v[24:39], v[52:55], v[164:167], v[24:39]
  ds_read_b128 v[44:47], v1 offset:29696
  s_waitcnt lgkmcnt(3)
  v_mfma_f32_32x32x16_bf16 v[24:39], v[56:59], v[168:171], v[24:39]
  ds_read_b128 v[48:51], v1 offset:30720
  s_add_u32 m0, s3, 4096
  s_add_u32 s57, s0, 4096
  buffer_load_dwordx4 v1, s[4:7], s57 offen sc1 lds
  s_waitcnt lgkmcnt(3)
  v_mfma_f32_32x32x16_bf16 v[24:39], v[60:63], v[172:175], v[24:39]
  ds_read_b128 v[52:55], v1 offset:31744
  s_waitcnt lgkmcnt(3)
  v_mfma_f32_32x32x16_bf16 v[24:39], v[40:43], v[176:179], v[24:39]
  ds_read_b128 v[56:59], v1 offset:32768
  buffer_load_dwordx4 v1, s[4:7], s57 offen offset:1024 sc1 lds
  s_waitcnt lgkmcnt(3)
  v_mfma_f32_32x32x16_bf16 v[24:39], v[44:47], v[180:183], v[24:39]
  ds_read_b128 v[60:63], v1 offset:33792
  s_waitcnt lgkmcnt(3)
  v_mfma_f32_32x32x16_bf16 v[24:39], v[48:51], v[184:187], v[24:39]
  ds_read_b128 v[40:43], v1 offset:34816
  buffer_load_dwordx4 v1, s[4:7], s57 offen offset:2048 sc1 lds
  s_waitcnt lgkmcnt(3)
  v_mfma_f32_32x32x16_bf16 v[24:39], v[52:55], v[188:191], v[24:39]
  ds_read_b128 v[44:47], v1 offset:35840
  s_waitcnt lgkmcnt(3)
  v_mfma_f32_32x32x16_bf16 v[24:39], v[56:59], v[192:195], v[24:39]
  ds_read_b128 v[48:51], v1 offset:36864
  s_waitcnt vmcnt(8)
  v_cmpx_eq_u32 vcc, s50, v97
  v_cmpx_eq_u32 vcc, s50, v99
  v_cmpx_eq_u32 vcc, s50, v101
  v_cmpx_eq_u32 vcc, s50, v103
  v_cmpx_eq_u32 vcc, s50, v105
  v_cmpx_eq_u32 vcc, s50, v107
  v_cmpx_eq_u32 vcc, s50, v109
  v_cmpx_eq_u32 vcc, s50, v111
  s_cmp_eq_u64 exec, -1
  s_mov_b64 exec, -1
  s_cbranch_scc1 .Legok_14
  s_movk_i32 s59, 0x7fff
.Lgpoll_15:
  buffer_load_dwordx4 v[96:99], v1, s[4:7], s16 offen sc1
  buffer_load_dwordx4 v[100:103], v1, s[4:7], s16 offen offset:1024 sc1
  buffer_load_dwordx4 v[104:107], v1, s[4:7], s16 offen offset:2048 sc1
  buffer_load_dwordx4 v[108:111], v1, s[4:7], s18 offen offset:3072 sc1
  s_waitcnt vmcnt(3)
  v_cmpx_eq_u32 vcc, s50, v97
  v_cmpx_eq_u32 vcc, s50, v99
  s_waitcnt vmcnt(2)
  v_cmpx_eq_u32 vcc, s50, v101
  v_cmpx_eq_u32 vcc, s50, v103
  s_waitcnt vmcnt(1)
  v_cmpx_eq_u32 vcc, s50, v105
  v_cmpx_eq_u32 vcc, s50, v107
  s_waitcnt vmcnt(0)
  v_cmpx_eq_u32 vcc, s50, v109
  v_cmpx_eq_u32 vcc, s50, v111
  s_cmp_eq_u64 exec, -1
  s_mov_b64 exec, -1
  s_cbranch_scc1 .Lgdone_16
  s_sleep 1
  s_sub_u32 s59, s59, 1
  s_cbranch_scc0 .Lgpoll_15
.Lgdone_16:
.Legok_14:
  v_add_f32 v96, v96, v100
  v_fmac_f32 v104, s17, v108
  v_add_f32 v96, v96, v104
  v_add_f32 v98, v98, v102
  v_fmac_f32 v106, s17, v110
  v_add_f32 v98, v98, v106
  v_cvt_pk_bf16_f32 v100, v96, v96
  v_and_b32 v100, 0xffff0000, v100
  v_sub_f32 v96, v96, v100
  v_cvt_pk_bf16_f32 v96, v100, v96
  v_cvt_pk_bf16_f32 v101, v98, v98
  v_and_b32 v101, 0xffff0000, v101
  v_sub_f32 v97, v98, v101
  v_cvt_pk_bf16_f32 v97, v101, v97
  ds_write_b64 v17, v[96:97] offset:2048
  buffer_load_dwordx4 v1, s[4:7], s57 offen offset:3072 sc1 lds
  s_waitcnt lgkmcnt(4)
  v_mfma_f32_32x32x16_bf16 v[24:39], v[60:63], v[196:199], v[24:39]
  ds_read_b128 v[52:55], v1 offset:37888
  s_waitcnt lgkmcnt(4)
  v_mfma_f32_32x32x16_bf16 v[24:39], v[40:43], v[200:203], v[24:39]
  ds_read_b128 v[56:59], v1 offset:38912
  s_add_u32 m0, s3, 8192
  s_add_u32 s57, s0, 8192
  buffer_load_dwordx4 v1, s[4:7], s57 offen sc1 lds
  s_waitcnt lgkmcnt(4)
  v_mfma_f32_32x32x16_bf16 v[24:39], v[44:47], v[204:207], v[24:39]
  ds_read_b128 v[60:63], v1 offset:39936
  s_waitcnt lgkmcnt(4)
  v_mfma_f32_32x32x16_bf16 v[24:39], v[48:51], v[208:211], v[24:39]
  ds_read_b128 v[40:43], v1 offset:40960
  buffer_load_dwordx4 v1, s[4:7], s57 offen offset:1024 sc1 lds
  s_waitcnt lgkmcnt(3)
  v_mfma_f32_32x32x16_bf16 v[24:39], v[52:55], v[212:215], v[24:39]
  ds_read_b128 v[44:47], v1 offset:41984
  s_waitcnt lgkmcnt(3)
  v_mfma_f32_32x32x16_bf16 v[24:39], v[56:59], v[216:219], v[24:39]
  ds_read_b128 v[48:51], v1 offset:43008
  buffer_load_dwordx4 v1, s[4:7], s57 offen offset:2048 sc1 lds
  s_waitcnt lgkmcnt(3)
  v_mfma_f32_32x32x16_bf16 v[24:39], v[60:63], v[220:223], v[24:39]
  ds_read_b128 v[52:55], v1 offset:44032
  s_waitcnt lgkmcnt(3)
  v_mfma_f32_32x32x16_bf16 v[24:39], v[40:43], v[224:227], v[24:39]
  buffer_load_dwordx4 v1, s[4:7], s57 offen offset:3072 sc1 lds
  s_waitcnt lgkmcnt(2)
  v_mfma_f32_32x32x16_bf16 v[24:39], v[44:47], v[228:231], v[24:39]
  s_waitcnt lgkmcnt(1)
  v_mfma_f32_32x32x16_bf16 v[24:39], v[48:51], v[232:235], v[24:39]
  s_waitcnt lgkmcnt(0)
  v_mfma_f32_32x32x16_bf16 v[24:39], v[52:55], v[236:239], v[24:39]
  v_mfma_f32_32x32x16_bf16 v[24:39], v[240:243], v[244:247], v[24:39]
  s_waitcnt vmcnt(12)
  s_branch .Lvend
.Lvp:
  s_waitcnt lgkmcnt(3)
  v_mfma_f32_32x32x16_bf16 v[24:39], v[40:43], v[128:131], 0
  ds_read_b128 v[56:59], v120 offset:32768
  buffer_store_dwordx4 v[252:255], v1, s[4:7], s40 offen sc1
  s_waitcnt lgkmcnt(3)
  v_mfma_f32_32x32x16_bf16 v[24:39], v[44:47], v[132:135], v[24:39]
  ds_read_b128 v[60:63], v120 offset:33792
  s_waitcnt lgkmcnt(3)
  v_mfma_f32_32x32x16_bf16 v[24:39], v[48:51], v[136:139], v[24:39]
  ds_read_b128 v[40:43], v120 offset:34816
  buffer_store_dwordx4 v[64:67], v1, s[4:7], s20 offen sc1
  s_waitcnt lgkmcnt(3)
  v_mfma_f32_32x32x16_bf16 v[24:39], v[52:55], v[140:143], v[24:39]
  ds_read_b128 v[44:47], v120 offset:35840
  s_waitcnt lgkmcnt(3)
  v_mfma_f32_32x32x16_bf16 v[24:39], v[56:59], v[144:147], v[24:39]
  ds_read_b128 v[48:51], v120 offset:36864
  buffer_store_dwordx4 v[68:71], v1, s[4:7], s20 offen offset:1024 sc1
  s_waitcnt lgkmcnt(3)
  v_mfma_f32_32x32x16_bf16 v[24:39], v[60:63], v[148:151], v[24:39]
  ds_read_b128 v[52:55], v120 offset:37888
  s_waitcnt lgkmcnt(3)
  v_mfma_f32_32x32x16_bf16 v[24:39], v[40:43], v[152:155], v[24:39]
  ds_read_b128 v[56:59], v120 offset:38912
  buffer_store_dwordx4 v[72:75], v1, s[4:7], s20 offen offset:2048 sc1
  s_waitcnt lgkmcnt(3)
  v_mfma_f32_32x32x16_bf16 v[24:39], v[44:47], v[156:159], v[24:39]
  ds_read_b128 v[60:63], v120 offset:39936
  s_waitcnt lgkmcnt(3)
  v_mfma_f32_32x32x16_bf16 v[24:39], v[48:51], v[160:163], v[24:39]
  s_waitcnt vmcnt(15)
  s_barrier
  ds_read_b128 v[40:43], v1 offset:28672
  buffer_store_dwordx4 v[76:79], v1, s[4:7], s20 offen offset:3072 sc1
  s_waitcnt lgkmcnt(3)
  v_mfma_f32_32x32x16_bf16 v[24:39], v[52:55], v[164:167], v[24:39]
  ds_read_b128 v[44:47], v1 offset:29696
  s_waitcnt lgkmcnt(3)
  v_mfma_f32_32x32x16_bf16 v[24:39], v[56:59], v[168:171], v[24:39]
  ds_read_b128 v[48:51], v1 offset:30720
  buffer_store_dwordx4 v[80:83], v1, s[4:7], s21 offen sc1
  s_waitcnt lgkmcnt(3)
  v_mfma_f32_32x32x16_bf16 v[24:39], v[60:63], v[172:175], v[24:39]
  ds_read_b128 v[52:55], v1 offset:31744
  s_waitcnt lgkmcnt(3)
  v_mfma_f32_32x32x16_bf16 v[24:39], v[40:43], v[176:179], v[24:39]
  ds_read_b128 v[56:59], v1 offset:32768
  buffer_store_dwordx4 v[84:87], v1, s[4:7], s21 offen offset:1024 sc1
  s_waitcnt lgkmcnt(3)
  v_mfma_f32_32x32x16_bf16 v[24:39], v[44:47], v[180:183], v[24:39]
  ds_read_b128 v[60:63], v1 offset:33792
  s_waitcnt lgkmcnt(3)
  v_mfma_f32_32x32x16_bf16 v[24:39], v[48:51], v[184:187], v[24:39]
  ds_read_b128 v[40:43], v1 offset:34816
  buffer_store_dwordx4 v[88:91], v1, s[4:7], s21 offen offset:2048 sc1
  s_waitcnt lgkmcnt(3)
  v_mfma_f32_32x32x16_bf16 v[24:39], v[52:55], v[188:191], v[24:39]
  ds_read_b128 v[44:47], v1 offset:35840
  s_waitcnt lgkmcnt(3)
  v_mfma_f32_32x32x16_bf16 v[24:39], v[56:59], v[192:195], v[24:39]
  ds_read_b128 v[48:51], v1 offset:36864
  s_waitcnt vmcnt(8)
  v_cmpx_eq_u32 vcc, s50, v97
  v_cmpx_eq_u32 vcc, s50, v99
  v_cmpx_eq_u32 vcc, s50, v101
  v_cmpx_eq_u32 vcc, s50, v103
  v_cmpx_eq_u32 vcc, s50, v105
  v_cmpx_eq_u32 vcc, s50, v107
  v_cmpx_eq_u32 vcc, s50, v109
  v_cmpx_eq_u32 vcc, s50, v111
  s_cmp_eq_u64 exec, -1
  s_mov_b64 exec, -1
  s_cbranch_scc1 .Legok_17
  s_movk_i32 s59, 0x7fff
.Lgpoll_18:
  buffer_load_dwordx4 v[96:99], v1, s[4:7], s16 offen sc1
  buffer_load_dwordx4 v[100:103], v1, s[4:7], s16 offen offset:1024 sc1
  buffer_load_dwordx4 v[104:107], v1, s[4:7], s16 offen offset:2048 sc1
  buffer_load_dwordx4 v[108:111], v1, s[4:7], s18 offen offset:3072 sc1
  s_waitcnt vmcnt(3)
  v_cmpx_eq_u32 vcc, s50, v97
  v_cmpx_eq_u32 vcc, s50, v99
  s_waitcnt vmcnt(2)
  v_cmpx_eq_u32 vcc, s50, v101
  v_cmpx_eq_u32 vcc, s50, v103
  s_waitcnt vmcnt(1)
  v_cmpx_eq_u32 vcc, s50, v105
  v_cmpx_eq_u32 vcc, s50, v107
  s_waitcnt vmcnt(0)
  v_cmpx_eq_u32 vcc, s50, v109
  v_cmpx_eq_u32 vcc, s50, v111
  s_cmp_eq_u64 exec, -1
  s_mov_b64 exec, -1
  s_cbranch_scc1 .Lgdone_19
  s_sleep 1
  s_sub_u32 s59, s59, 1
  s_cbranch_scc0 .Lgpoll_18
.Lgdone_19:
.Legok_17:
  v_add_f32 v96, v96, v100
  v_fmac_f32 v104, s17, v108
  v_add_f32 v96, v96, v104
  v_add_f32 v98, v98, v102
  v_fmac_f32 v106, s17, v110
  v_add_f32 v98, v98, v106
  v_cvt_pk_bf16_f32 v100, v96, v96
  v_and_b32 v100, 0xffff0000, v100
  v_sub_f32 v96, v96, v100
  v_cvt_pk_bf16_f32 v96, v100, v96
  v_cvt_pk_bf16_f32 v101, v98, v98
  v_and_b32 v101, 0xffff0000, v101
  v_sub_f32 v97, v98, v101
  v_cvt_pk_bf16_f32 v97, v101, v97
  ds_write_b64 v17, v[96:97] offset:2048
  buffer_store_dwordx4 v[92:95], v1, s[4:7], s21 offen offset:3072 sc1
  s_waitcnt lgkmcnt(4)
  v_mfma_f32_32x32x16_bf16 v[24:39], v[60:63], v[196:199], v[24:39]
  ds_read_b128 v[52:55], v1 offset:37888
  s_waitcnt lgkmcnt(4)
  v_mfma_f32_32x32x16_bf16 v[24:39], v[40:43], v[200:203], v[24:39]
  ds_read_b128 v[56:59], v1 offset:38912
  s_waitcnt lgkmcnt(4)
  v_mfma_f32_32x32x16_bf16 v[24:39], v[44:47], v[204:207], v[24:39]
  ds_read_b128 v[60:63], v1 offset:39936
  s_waitcnt lgkmcnt(4)
  v_mfma_f32_32x32x16_bf16 v[24:39], v[48:51], v[208:211], v[24:39]
  ds_read_b128 v[40:43], v1 offset:40960
  s_waitcnt lgkmcnt(3)
  v_mfma_f32_32x32x16_bf16 v[24:39], v[52:55], v[212:215], v[24:39]
  ds_read_b128 v[44:47], v1 offset:41984
  s_waitcnt lgkmcnt(3)
  v_mfma_f32_32x32x16_bf16 v[24:39], v[56:59], v[216:219], v[24:39]
  ds_read_b128 v[48:51], v1 offset:43008
  s_waitcnt lgkmcnt(3)
  v_mfma_f32_32x32x16_bf16 v[24:39], v[60:63], v[220:223], v[24:39]
  ds_read_b128 v[52:55], v1 offset:44032
  s_waitcnt lgkmcnt(3)
  v_mfma_f32_32x32x16_bf16 v[24:39], v[40:43], v[224:227], v[24:39]
  s_waitcnt lgkmcnt(2)
  v_mfma_f32_32x32x16_bf16 v[24:39], v[44:47], v[228:231], v[24:39]
  s_waitcnt lgkmcnt(1)
  v_mfma_f32_32x32x16_bf16 v[24:39], v[48:51], v[232:235], v[24:39]
  s_waitcnt lgkmcnt(0)
  v_mfma_f32_32x32x16_bf16 v[24:39], v[52:55], v[236:239], v[24:39]
  v_mfma_f32_32x32x16_bf16 v[24:39], v[240:243], v[244:247], v[24:39]
  s_waitcnt vmcnt(8)
.Lvend:
  s_nop 10
  v_pk_mul_f32 v[24:25], v[24:25], v[112:113] op_sel_hi:[1,0]
  v_pk_mul_f32 v[26:27], v[26:27], v[112:113] op_sel_hi:[1,0]
  v_pk_mul_f32 v[28:29], v[28:29], v[112:113] op_sel_hi:[1,0]
  v_pk_mul_f32 v[30:31], v[30:31], v[112:113] op_sel_hi:[1,0]
  v_pk_mul_f32 v[32:33], v[32:33], v[112:113] op_sel_hi:[1,0]
  v_pk_mul_f32 v[34:35], v[34:35], v[112:113] op_sel_hi:[1,0]
  v_pk_mul_f32 v[36:37], v[36:37], v[112:113] op_sel_hi:[1,0]
  v_cvt_pk_bf16_f32 v40, v24, v25
  v_cvt_pk_bf16_f32 v41, v26, v27
  v_cvt_pk_bf16_f32 v42, v28, v29
  v_cvt_pk_bf16_f32 v43, v30, v31
  v_cvt_pk_bf16_f32 v44, v32, v33
  v_cvt_pk_bf16_f32 v45, v34, v35
  v_cvt_pk_bf16_f32 v46, v36, v37
  v_mov_b32 v47, 0x3f80
  ds_write_b64 v116, v[40:41] offset:16384
  ds_write_b64 v117, v[42:43] offset:16384
  ds_write_b64 v118, v[44:45] offset:16384
  ds_write_b64 v119, v[46:47] offset:16384
  s_setprio 1
  s_waitcnt lgkmcnt(0)
  s_barrier
  ds_read_b64_tr_b16 v[16:17], v5
  ds_read_b64_tr_b16 v[18:19], v5 offset:1024
  ds_read_b64_tr_b16 v[20:21], v6 offset:16384
  ds_read_b64_tr_b16 v[22:23], v6 offset:17408
  ds_read_b64_tr_b16 v[24:25], v7 offset:16384
  ds_read_b64_tr_b16 v[26:27], v7 offset:17408
  ds_read_b64_tr_b16 v[28:29], v5 offset:2048
  ds_read_b64_tr_b16 v[30:31], v5 offset:3072
  ds_read_b64_tr_b16 v[32:33], v6 offset:18432
  ds_read_b64_tr_b16 v[34:35], v6 offset:19456
  ds_read_b64_tr_b16 v[36:37], v7 offset:18432
  ds_read_b64_tr_b16 v[38:39], v7 offset:19456
  s_waitcnt lgkmcnt(6)
  v_mfma_f32_16x16x32_bf16 v[8:11], v[16:19], v[20:23], 0
  v_mfma_f32_16x16x32_bf16 v[12:15], v[16:19], v[24:27], 0
  ds_read_b64_tr_b16 v[40:41], v5 offset:4096
  ds_read_b64_tr_b16 v[42:43], v5 offset:5120
  ds_read_b64_tr_b16 v[44:45], v6 offset:20480
  ds_read_b64_tr_b16 v[46:47], v6 offset:21504
  ds_read_b64_tr_b16 v[48:49], v7 offset:20480
  ds_read_b64_tr_b16 v[50:51], v7 offset:21504
  s_waitcnt lgkmcnt(6)
  v_mfma_f32_16x16x32_bf16 v[8:11], v[28:31], v[32:35], v[8:11]
  v_mfma_f32_16x16x32_bf16 v[12:15], v[28:31], v[36:39], v[12:15]
  ds_read_b64_tr_b16 v[52:53], v5 offset:6144
  ds_read_b64_tr_b16 v[54:55], v5 offset:7168
  ds_read_b64_tr_b16 v[56:57], v6 offset:22528
  ds_read_b64_tr_b16 v[58:59], v6 offset:23552
  ds_read_b64_tr_b16 v[60:61], v7 offset:22528
  ds_read_b64_tr_b16 v[62:63], v7 offset:23552
  s_waitcnt lgkmcnt(6)
  v_mfma_f32_16x16x32_bf16 v[8:11], v[40:43], v[44:47], v[8:11]
  v_mfma_f32_16x16x32_bf16 v[12:15], v[40:43], v[48:51], v[12:15]
  s_waitcnt lgkmcnt(0)
  v_mfma_f32_16x16x32_bf16 v[8:11], v[52:55], v[56:59], v[8:11]
  v_mfma_f32_16x16x32_bf16 v[12:15], v[52:55], v[60:63], v[12:15]
  s_nop 6
  v_cvt_pk_bf16_f32 v16, v8, v9
  v_cvt_pk_bf16_f32 v17, v10, v11
  v_cvt_pk_bf16_f32 v18, v12, v13
  v_cvt_pk_bf16_f32 v19, v14, v15
  buffer_store_dwordx4 v[16:19], v1, s[4:7], s40 offen sc1
  buffer_store_dword v2, v115, s[4:7], s41 offen sc1
  s_setprio 0
  buffer_store_dwordx4 v[252:255], v121, s[4:7], s42 offen sc1
  buffer_store_dwordx4 v[252:255], v121, s[4:7], s42 offen offset:512 sc1
  s_cmp_ge_u32 s56, 28
  s_cbranch_scc1 .Lh1
  s_mov_b32 s8, s24
  s_and_b32 s9, s25, 0xffff
  s_mov_b32 s10, 1835008
  s_mov_b32 s11, 0x20000
  s_lshl1_add_u32 s43, s54, s55
  s_lshl2_add_u32 s43, s43, s49
  v_lshrrev_b32 v8, 3, v0
  v_mul_u32_u24 v8, 896, v8
  v_and_b32 v6, 0x70, v1
  v_or_b32 v8, v8, v6
  s_mul_i32 s57, s43, 14336
  s_lshl_b32 s58, s53, 7
  s_add_u32 s57, s57, s58
  s_lshl_b32 s14, s49, 11
  s_add_u32 m0, s14, 28672
  s_add_u32 s58, s57, 6144
  buffer_load_dwordx4 v8, s[8:11], s57 offen lds
  buffer_load_dwordx4 v8, s[8:11], s58 offen offset:1024 lds
  s_min_u32 s57, s49, 2
  s_lshl_b32 s1, s57, 8
  s_add_u32 s1, s1, 162816
  s_mul_i32 s58, s57, 7
  s_cmp_eq_u32 s57, 0
  s_cselect_b32 s12, 0, 7
  s_add_u32 s12, s12, s58
  s_add_u32 s12, s12, s53
  s_lshl_b32 s12, s12, 9
  s_add_u32 s12, s12, 1290240
  s_cmp_lt_u32 s56, 14
  s_cselect_b32 s13, s32, 0
  s_lshl_b32 s13, s13, 9
  s_add_u32 s13, s13, 1293824
  v_lshrrev_b32 v6, 5, v0
  v_lshlrev_b32 v5, 2, v3
  v_mul_u32_u24 v7, 3584, v6
  v_add_u32 v7, v7, v5
  buffer_load_dwordx4 v[24:27], v5, s[4:7], s12 offen sc1
  buffer_load_dwordx4 v[28:31], v7, s[4:7], s13 offen sc1
  s_cmp_ge_u32 s2, 98
  s_cbranch_scc1 .Lbpre1
  v_cmp_gt_u32 vcc, 2, v0
  v_lshlrev_b32 v12, 5, v0
  s_waitcnt vmcnt(8)
  v_cndmask_b32 v12, v4, v12, vcc
  buffer_store_dword v2, v12, s[4:7], s22 offen sc1
  s_add_u32 m0, s3, 0
  s_add_u32 s57, s0, 0
  buffer_load_dwordx4 v1, s[4:7], s57 offen sc1 lds
  buffer_load_dwordx4 v1, s[4:7], s57 offen offset:1024 sc1 lds
  buffer_load_dwordx4 v1, s[4:7], s57 offen offset:2048 sc1 lds
  buffer_load_dwordx4 v1, s[4:7], s57 offen offset:3072 sc1 lds
  s_add_u32 m0, s3, 4096
  s_add_u32 s57, s0, 4096
  buffer_load_dwordx4 v1, s[4:7], s57 offen sc1 lds
  buffer_load_dwordx4 v1, s[4:7], s57 offen offset:1024 sc1 lds
  buffer_load_dwordx4 v1, s[4:7], s57 offen offset:2048 sc1 lds
  buffer_load_dwordx4 v1, s[4:7], s57 offen offset:3072 sc1 lds
  s_add_u32 m0, s3, 8192
  s_add_u32 s57, s0, 8192
  buffer_load_dwordx4 v1, s[4:7], s57 offen sc1 lds
  buffer_load_dwordx4 v1, s[4:7], s57 offen offset:1024 sc1 lds
  buffer_load_dwordx4 v1, s[4:7], s57 offen offset:2048 sc1 lds
  buffer_load_dwordx4 v1, s[4:7], s57 offen offset:3072 sc1 lds
.Lbpre1:
  s_add_u32 m0, s3, 12288
  s_add_u32 s57, s0, 12288
  buffer_load_dwordx4 v1, s[4:7], s57 offen sc1 lds
  buffer_load_dwordx4 v1, s[4:7], s57 offen offset:1024 sc1 lds
  buffer_load_dwordx4 v1, s[4:7], s57 offen offset:2048 sc1 lds
  buffer_load_dwordx4 v1, s[4:7], s57 offen offset:3072 sc1 lds
  v_and_b32 v9, 15, v0
  v_lshrrev_b32 v10, 4, v0
  v_lshlrev_b32 v11, 5, v10
  v_lshl_or_b32 v246, v9, 7, v11
  v_add_u32 v246, s14, v246
  v_lshlrev_b32 v247, 6, v10
  v_add_u32 v247, 162816, v247
  v_add_u32 v245, 73728, v1
  v_lshlrev_b32 v249, 4, v9
  v_lshl_or_b32 v248, v10, 10, v249
  v_cmp_gt_u32 vcc, 16, v0
  s_nop 1
  v_cndmask_b32 v249, v4, v249, vcc
  s_waitcnt vmcnt(4)
  ds_read_b128 v[8:11], v246 offset:28672
  ds_read_b128 v[12:15], v246 offset:28688
  v_cmpx_eq_u32 vcc, s50, v25
  v_cmpx_eq_u32 vcc, s50, v27
  v_cmpx_eq_u32 vcc, s50, v29
  v_cmpx_eq_u32 vcc, s50, v31
  s_cmp_eq_u64 exec, -1
  s_mov_b64 exec, -1
  s_cbranch_scc1 .Lvecok
  s_movk_i32 s59, 0x7fff
.Lgpoll_20:
  buffer_load_dwordx4 v[24:27], v5, s[4:7], s12 offen sc1
  buffer_load_dwordx4 v[28:31], v7, s[4:7], s13 offen sc1
  s_waitcnt vmcnt(1)
  v_cmpx_eq_u32 vcc, s50, v25
  v_cmpx_eq_u32 vcc, s50, v27
  s_waitcnt vmcnt(0)
  v_cmpx_eq_u32 vcc, s50, v29
  v_cmpx_eq_u32 vcc, s50, v31
  s_cmp_eq_u64 exec, -1
  s_mov_b64 exec, -1
  s_cbranch_scc1 .Lgdone_21
  s_sleep 1
  s_sub_u32 s59, s59, 1
  s_cbranch_scc0 .Lgpoll_20
.Lgdone_21:
.Lvecok:
  v_lshl_add_u32 v5, v3, 1, s1
  ds_write2_b32 v5, v24, v26 offset0:0 offset1:1
  v_cvt_pk_bf16_f32 v28, v28, v30
  v_lshl_add_u32 v5, v6, 7, v3
  v_add_u32 v5, 163584, v5
  ds_write_b32 v5, v28
  v_add_u32 v246, 154624, v246
  s_waitcnt lgkmcnt(2)
  v_lshlrev_b32 v112, 16, v8
  v_and_b32 v113, 0xffff0000, v8
  v_lshlrev_b32 v114, 16, v9
  v_and_b32 v115, 0xffff0000, v9
  v_lshlrev_b32 v116, 16, v10
  v_and_b32 v117, 0xffff0000, v10
  v_lshlrev_b32 v118, 16, v11
  v_and_b32 v119, 0xffff0000, v11
  v_lshlrev_b32 v120, 16, v12
  v_and_b32 v121, 0xffff0000, v12
  v_lshlrev_b32 v122, 16, v13
  v_and_b32 v123, 0xffff0000, v13
  v_lshlrev_b32 v124, 16, v14
  v_and_b32 v125, 0xffff0000, v14
  v_lshlrev_b32 v126, 16, v15
  v_and_b32 v127, 0xffff0000, v15
  v_add_f32 v8, v112, v113
  v_add_f32 v9, v114, v115
  v_add_f32 v10, v116, v117
  v_add_f32 v11, v118, v119
  v_add_f32 v12, v120, v121
  v_add_f32 v13, v122, v123
  v_add_f32 v14, v124, v125
  v_add_f32 v15, v126, v127
  v_add_f32 v8, v8, v12
  v_add_f32 v9, v9, v13
  v_add_f32 v10, v10, v14
  v_add_f32 v11, v11, v15
  v_add_f32 v8, v8, v10
  v_add_f32 v9, v9, v11
  v_add_f32 v16, v8, v9
  v_mov_b32 v17, v16
  s_nop 1
  v_permlane16_swap_b32 v16, v17
  v_add_f32 v16, v16, v17
  v_mov_b32 v17, v16
  s_nop 1
  v_permlane32_swap_b32 v16, v17
  v_add_f32 v16, v16, v17
  v_mul_f32 v16, 0x3c800000, v16
  v_cvt_pk_bf16_f32 v244, v16, v16
  v_and_b32 v244, 0xffff0000, v244
  v_sub_f32 v112, v112, v244
  v_sub_f32 v113, v113, v244
  v_sub_f32 v114, v114, v244
  v_sub_f32 v115, v115, v244
  v_sub_f32 v116, v116, v244
  v_sub_f32 v117, v117, v244
  v_sub_f32 v118, v118, v244
  v_sub_f32 v119, v119, v244
  v_sub_f32 v120, v120, v244
  v_sub_f32 v121, v121, v244
  v_sub_f32 v122, v122, v244
  v_sub_f32 v123, v123, v244
  v_sub_f32 v124, v124, v244
  v_sub_f32 v125, v125, v244
  v_sub_f32 v126, v126, v244
  v_sub_f32 v127, v127, v244
  s_branch .Lh1
.LA:
  buffer_load_dwordx4 v80, s[8:11], s0 offen lds
  buffer_load_dwordx4 v81, s[8:11], s0 offen offset:1024 lds
  buffer_load_dwordx4 v82, s[8:11], s0 offen offset:2048 lds
  buffer_load_dwordx4 v83, s[8:11], s0 offen offset:3072 lds
  s_add_u32 m0, m0, 4096
  s_add_u32 s0, s0, 128
  s_cmp_ge_u32 s0, s33
  s_cselect_b32 s57, 896, 0
  s_sub_u32 s0, s0, s57
  buffer_load_dwordx4 v80, s[8:11], s0 offen lds
  buffer_load_dwordx4 v81, s[8:11], s0 offen offset:1024 lds
  buffer_load_dwordx4 v82, s[8:11], s0 offen offset:2048 lds
  buffer_load_dwordx4 v83, s[8:11], s0 offen offset:3072 lds
  v_and_b32 v12, 31, v0
  v_bfe_u32 v13, v0, 1, 3
  v_lshrrev_b32 v14, 5, v0
  v_xor_b32 v13, v13, v14
  v_lshlrev_b32 v13, 4, v13
  v_lshl_or_b32 v12, v12, 7, v13
  s_mul_i32 s57, s49, 28672
  v_add_u32 v12, s57, v12
  v_xor_b32 v13, 32, v12
  v_xor_b32 v14, 64, v12
  v_xor_b32 v15, 96, v12
  v_mov_b32 v16, 0
  v_mov_b32 v17, 0
  s_mov_b32 s58, 0x3f803f80
  s_waitcnt vmcnt(4)
  ds_read_b128 v[128:131], v12
  ds_read_b128 v[132:135], v13
  ds_read_b128 v[136:139], v14
  ds_read_b128 v[140:143], v15
  s_add_u32 m0, m0, 4096
  s_add_u32 s0, s0, 128
  s_cmp_ge_u32 s0, s33
  s_cselect_b32 s57, 896, 0
  s_sub_u32 s0, s0, s57
  buffer_load_dwordx4 v80, s[8:11], s0 offen lds
  buffer_load_dwordx4 v81, s[8:11], s0 offen offset:1024 lds
  buffer_load_dwordx4 v82, s[8:11], s0 offen offset:2048 lds
  buffer_load_dwordx4 v83, s[8:11], s0 offen offset:3072 lds
  s_waitcnt vmcnt(4)
  ds_read_b128 v[144:147], v12 offset:4096
  ds_read_b128 v[148:151], v13 offset:4096
  ds_read_b128 v[152:155], v14 offset:4096
  ds_read_b128 v[156:159], v15 offset:4096
  s_waitcnt lgkmcnt(4)
  s_add_u32 m0, m0, 4096
  s_add_u32 s0, s0, 128
  s_cmp_ge_u32 s0, s33
  s_cselect_b32 s57, 896, 0
  s_sub_u32 s0, s0, s57
  buffer_load_dwordx4 v80, s[8:11], s0 offen lds
  v_dot2c_f32_bf16 v16, s58, v128
  v_dot2c_f32_bf16 v17, v128, v128
  v_dot2c_f32_bf16 v16, s58, v129
  v_dot2c_f32_bf16 v17, v129, v129
  v_dot2c_f32_bf16 v16, s58, v130
  v_dot2c_f32_bf16 v17, v130, v130
  v_dot2c_f32_bf16 v16, s58, v131
  v_dot2c_f32_bf16 v17, v131, v131
  v_dot2c_f32_bf16 v16, s58, v132
  buffer_load_dwordx4 v81, s[8:11], s0 offen offset:1024 lds
  v_dot2c_f32_bf16 v17, v132, v132
  v_dot2c_f32_bf16 v16, s58, v133
  v_dot2c_f32_bf16 v17, v133, v133
  v_dot2c_f32_bf16 v16, s58, v134
  v_dot2c_f32_bf16 v17, v134, v134
  v_dot2c_f32_bf16 v16, s58, v135
  v_dot2c_f32_bf16 v17, v135, v135
  v_dot2c_f32_bf16 v16, s58, v136
  v_dot2c_f32_bf16 v17, v136, v136
  buffer_load_dwordx4 v82, s[8:11], s0 offen offset:2048 lds
  v_dot2c_f32_bf16 v16, s58, v137
  v_dot2c_f32_bf16 v17, v137, v137
  v_dot2c_f32_bf16 v16, s58, v138
  v_dot2c_f32_bf16 v17, v138, v138
  v_dot2c_f32_bf16 v16, s58, v139
  v_dot2c_f32_bf16 v17, v139, v139
  v_dot2c_f32_bf16 v16, s58, v140
  v_dot2c_f32_bf16 v17, v140, v140
  v_dot2c_f32_bf16 v16, s58, v141
  buffer_load_dwordx4 v83, s[8:11], s0 offen offset:3072 lds
  v_dot2c_f32_bf16 v17, v141, v141
  v_dot2c_f32_bf16 v16, s58, v142
  v_dot2c_f32_bf16 v17, v142, v142
  v_dot2c_f32_bf16 v16, s58, v143
  v_dot2c_f32_bf16 v17, v143, v143
  s_waitcnt vmcnt(4)
  ds_read_b128 v[160:163], v12 offset:8192
  ds_read_b128 v[164:167], v13 offset:8192
  ds_read_b128 v[168:171], v14 offset:8192
  ds_read_b128 v[172:175], v15 offset:8192
  s_sub_u32 s57, s56, 14
  s_cmp_ge_u32 s57, 14
  s_cbranch_scc1 .Lnopause
  s_barrier
.Lnopause:
  s_waitcnt lgkmcnt(4)
  s_add_u32 m0, m0, 4096
  s_add_u32 s0, s0, 128
  s_cmp_ge_u32 s0, s33
  s_cselect_b32 s57, 896, 0
  s_sub_u32 s0, s0, s57
  buffer_load_dwordx4 v80, s[8:11], s0 offen lds
  v_dot2c_f32_bf16 v16, s58, v144
  v_dot2c_f32_bf16 v17, v144, v144
  v_dot2c_f32_bf16 v16, s58, v145
  v_dot2c_f32_bf16 v17, v145, v145
  v_dot2c_f32_bf16 v16, s58, v146
  v_dot2c_f32_bf16 v17, v146, v146
  v_dot2c_f32_bf16 v16, s58, v147
  v_dot2c_f32_bf16 v17, v147, v147
  v_dot2c_f32_bf16 v16, s58, v148
  buffer_load_dwordx4 v81, s[8:11], s0 offen offset:1024 lds
  v_dot2c_f32_bf16 v17, v148, v148
  v_dot2c_f32_bf16 v16, s58, v149
  v_dot2c_f32_bf16 v17, v149, v149
  v_dot2c_f32_bf16 v16, s58, v150
  v_dot2c_f32_bf16 v17, v150, v150
  v_dot2c_f32_bf16 v16, s58, v151
  v_dot2c_f32_bf16 v17, v151, v151
  v_dot2c_f32_bf16 v16, s58, v152
  v_dot2c_f32_bf16 v17, v152, v152
  buffer_load_dwordx4 v82, s[8:11], s0 offen offset:2048 lds
  v_dot2c_f32_bf16 v16, s58, v153
  v_dot2c_f32_bf16 v17, v153, v153
  v_dot2c_f32_bf16 v16, s58, v154
  v_dot2c_f32_bf16 v17, v154, v154
  v_dot2c_f32_bf16 v16, s58, v155
  v_dot2c_f32_bf16 v17, v155, v155
  v_dot2c_f32_bf16 v16, s58, v156
  v_dot2c_f32_bf16 v17, v156, v156
  v_dot2c_f32_bf16 v16, s58, v157
  buffer_load_dwordx4 v83, s[8:11], s0 offen offset:3072 lds
  v_dot2c_f32_bf16 v17, v157, v157
  v_dot2c_f32_bf16 v16, s58, v158
  v_dot2c_f32_bf16 v17, v158, v158
  v_dot2c_f32_bf16 v16, s58, v159
  v_dot2c_f32_bf16 v17, v159, v159
  s_waitcnt vmcnt(4)
  ds_read_b128 v[176:179], v12 offset:12288
  ds_read_b128 v[180:183], v13 offset:12288
  ds_read_b128 v[184:187], v14 offset:12288
  ds_read_b128 v[188:191], v15 offset:12288
  s_waitcnt lgkmcnt(4)
  s_add_u32 m0, m0, 4096
  s_add_u32 s0, s0, 128
  s_cmp_ge_u32 s0, s33
  s_cselect_b32 s57, 896, 0
  s_sub_u32 s0, s0, s57
  buffer_load_dwordx4 v80, s[8:11], s0 offen lds
  v_dot2c_f32_bf16 v16, s58, v160
  v_dot2c_f32_bf16 v17, v160, v160
  v_dot2c_f32_bf16 v16, s58, v161
  v_dot2c_f32_bf16 v17, v161, v161
  v_dot2c_f32_bf16 v16, s58, v162
  v_dot2c_f32_bf16 v17, v162, v162
  v_dot2c_f32_bf16 v16, s58, v163
  v_dot2c_f32_bf16 v17, v163, v163
  v_dot2c_f32_bf16 v16, s58, v164
  buffer_load_dwordx4 v81, s[8:11], s0 offen offset:1024 lds
  v_dot2c_f32_bf16 v17, v164, v164
  v_dot2c_f32_bf16 v16, s58, v165
  v_dot2c_f32_bf16 v17, v165, v165
  v_dot2c_f32_bf16 v16, s58, v166
  v_dot2c_f32_bf16 v17, v166, v166
  v_dot2c_f32_bf16 v16, s58, v167
  v_dot2c_f32_bf16 v17, v167, v167
  v_dot2c_f32_bf16 v16, s58, v168
  v_dot2c_f32_bf16 v17, v168, v168
  buffer_load_dwordx4 v82, s[8:11], s0 offen offset:2048 lds
  v_dot2c_f32_bf16 v16, s58, v169
  v_dot2c_f32_bf16 v17, v169, v169
  v_dot2c_f32_bf16 v16, s58, v170
  v_dot2c_f32_bf16 v17, v170, v170
  v_dot2c_f32_bf16 v16, s58, v171
  v_dot2c_f32_bf16 v17, v171, v171
  v_dot2c_f32_bf16 v16, s58, v172
  v_dot2c_f32_bf16 v17, v172, v172
  v_dot2c_f32_bf16 v16, s58, v173
  buffer_load_dwordx4 v83, s[8:11], s0 offen offset:3072 lds
  v_dot2c_f32_bf16 v17, v173, v173
  v_dot2c_f32_bf16 v16, s58, v174
  v_dot2c_f32_bf16 v17, v174, v174
  v_dot2c_f32_bf16 v16, s58, v175
  v_dot2c_f32_bf16 v17, v175, v175
  s_waitcnt vmcnt(4)
  ds_read_b128 v[192:195], v12 offset:16384
  ds_read_b128 v[196:199], v13 offset:16384
  ds_read_b128 v[200:203], v14 offset:16384
  ds_read_b128 v[204:207], v15 offset:16384
  s_waitcnt lgkmcnt(4)
  s_add_u32 m0, m0, 4096
  s_add_u32 s0, s0, 128
  s_cmp_ge_u32 s0, s33
  s_cselect_b32 s57, 896, 0
  s_sub_u32 s0, s0, s57
  buffer_load_dwordx4 v80, s[8:11], s0 offen lds
  v_dot2c_f32_bf16 v16, s58, v176
  v_dot2c_f32_bf16 v17, v176, v176
  v_dot2c_f32_bf16 v16, s58, v177
  v_dot2c_f32_bf16 v17, v177, v177
  v_dot2c_f32_bf16 v16, s58, v178
  v_dot2c_f32_bf16 v17, v178, v178
  v_dot2c_f32_bf16 v16, s58, v179
  v_dot2c_f32_bf16 v17, v179, v179
  v_dot2c_f32_bf16 v16, s58, v180
  buffer_load_dwordx4 v81, s[8:11], s0 offen offset:1024 lds
  v_dot2c_f32_bf16 v17, v180, v180
  v_dot2c_f32_bf16 v16, s58, v181
  v_dot2c_f32_bf16 v17, v181, v181
  v_dot2c_f32_bf16 v16, s58, v182
  v_dot2c_f32_bf16 v17, v182, v182
  v_dot2c_f32_bf16 v16, s58, v183
  v_dot2c_f32_bf16 v17, v183, v183
  v_dot2c_f32_bf16 v16, s58, v184
  v_dot2c_f32_bf16 v17, v184, v184
  buffer_load_dwordx4 v82, s[8:11], s0 offen offset:2048 lds
  v_dot2c_f32_bf16 v16, s58, v185
  v_dot2c_f32_bf16 v17, v185, v185
  v_dot2c_f32_bf16 v16, s58, v186
  v_dot2c_f32_bf16 v17, v186, v186
  v_dot2c_f32_bf16 v16, s58, v187
  v_dot2c_f32_bf16 v17, v187, v187
  v_dot2c_f32_bf16 v16, s58, v188
  v_dot2c_f32_bf16 v17, v188, v188
  v_dot2c_f32_bf16 v16, s58, v189
  buffer_load_dwordx4 v83, s[8:11], s0 offen offset:3072 lds
  v_dot2c_f32_bf16 v17, v189, v189
  v_dot2c_f32_bf16 v16, s58, v190
  v_dot2c_f32_bf16 v17, v190, v190
  v_dot2c_f32_bf16 v16, s58, v191
  v_dot2c_f32_bf16 v17, v191, v191
  s_waitcnt vmcnt(4)
  ds_read_b128 v[208:211], v12 offset:20480
  ds_read_b128 v[212:215], v13 offset:20480
  ds_read_b128 v[216:219], v14 offset:20480
  ds_read_b128 v[220:223], v15 offset:20480
  s_waitcnt lgkmcnt(4)
  v_dot2c_f32_bf16 v16, s58, v192
  v_dot2c_f32_bf16 v17, v192, v192
  v_dot2c_f32_bf16 v16, s58, v193
  v_dot2c_f32_bf16 v17, v193, v193
  v_dot2c_f32_bf16 v16, s58, v194
  v_dot2c_f32_bf16 v17, v194, v194
  v_dot2c_f32_bf16 v16, s58, v195
  v_dot2c_f32_bf16 v17, v195, v195
  v_dot2c_f32_bf16 v16, s58, v196
  v_dot2c_f32_bf16 v17, v196, v196
  v_dot2c_f32_bf16 v16, s58, v197
  v_dot2c_f32_bf16 v17, v197, v197
  v_dot2c_f32_bf16 v16, s58, v198
  v_dot2c_f32_bf16 v17, v198, v198
  v_dot2c_f32_bf16 v16, s58, v199
  v_dot2c_f32_bf16 v17, v199, v199
  v_dot2c_f32_bf16 v16, s58, v200
  v_dot2c_f32_bf16 v17, v200, v200
  v_dot2c_f32_bf16 v16, s58, v201
  v_dot2c_f32_bf16 v17, v201, v201
  v_dot2c_f32_bf16 v16, s58, v202
  v_dot2c_f32_bf16 v17, v202, v202
  v_dot2c_f32_bf16 v16, s58, v203
  v_dot2c_f32_bf16 v17, v203, v203
  v_dot2c_f32_bf16 v16, s58, v204
  v_dot2c_f32_bf16 v17, v204, v204
  v_dot2c_f32_bf16 v16, s58, v205
  v_dot2c_f32_bf16 v17, v205, v205
  v_dot2c_f32_bf16 v16, s58, v206
  v_dot2c_f32_bf16 v17, v206, v206
  v_dot2c_f32_bf16 v16, s58, v207
  v_dot2c_f32_bf16 v17, v207, v207
  s_waitcnt vmcnt(0)
  ds_read_b128 v[224:227], v12 offset:24576
  ds_read_b128 v[228:231], v13 offset:24576
  ds_read_b128 v[232:235], v14 offset:24576
  ds_read_b128 v[236:239], v15 offset:24576
  s_waitcnt lgkmcnt(4)
  v_dot2c_f32_bf16 v16, s58, v208
  v_dot2c_f32_bf16 v17, v208, v208
  v_dot2c_f32_bf16 v16, s58, v209
  v_dot2c_f32_bf16 v17, v209, v209
  v_dot2c_f32_bf16 v16, s58, v210
  v_dot2c_f32_bf16 v17, v210, v210
  v_dot2c_f32_bf16 v16, s58, v211
  v_dot2c_f32_bf16 v17, v211, v211
  v_dot2c_f32_bf16 v16, s58, v212
  v_dot2c_f32_bf16 v17, v212, v212
  v_dot2c_f32_bf16 v16, s58, v213
  v_dot2c_f32_bf16 v17, v213, v213
  v_dot2c_f32_bf16 v16, s58, v214
  v_dot2c_f32_bf16 v17, v214, v214
  v_dot2c_f32_bf16 v16, s58, v215
  v_dot2c_f32_bf16 v17, v215, v215
  v_dot2c_f32_bf16 v16, s58, v216
  v_dot2c_f32_bf16 v17, v216, v216
  v_dot2c_f32_bf16 v16, s58, v217
  v_dot2c_f32_bf16 v17, v217, v217
  v_dot2c_f32_bf16 v16, s58, v218
  v_dot2c_f32_bf16 v17, v218, v218
  v_dot2c_f32_bf16 v16, s58, v219
  v_dot2c_f32_bf16 v17, v219, v219
  v_dot2c_f32_bf16 v16, s58, v220
  v_dot2c_f32_bf16 v17, v220, v220
  v_dot2c_f32_bf16 v16, s58, v221
  v_dot2c_f32_bf16 v17, v221, v221
  v_dot2c_f32_bf16 v16, s58, v222
  v_dot2c_f32_bf16 v17, v222, v222
  v_dot2c_f32_bf16 v16, s58, v223
  v_dot2c_f32_bf16 v17, v223, v223
  s_waitcnt lgkmcnt(0)
  s_barrier
  s_cmp_lt_u32 s56, 14
  s_cbranch_scc1 .Ltagd
  s_mul_i32 s50, s50, 1664525
  s_add_u32 s50, s50, 1013904223
  v_mov_b32 v2, s50
.Ltagd:
  v_dot2c_f32_bf16 v16, s58, v224
  v_dot2c_f32_bf16 v17, v224, v224
  v_dot2c_f32_bf16 v16, s58, v225
  v_dot2c_f32_bf16 v17, v225, v225
  v_dot2c_f32_bf16 v16, s58, v226
  v_dot2c_f32_bf16 v17, v226, v226
  v_dot2c_f32_bf16 v16, s58, v227
  v_dot2c_f32_bf16 v17, v227, v227
  v_dot2c_f32_bf16 v16, s58, v228
  v_dot2c_f32_bf16 v17, v228, v228
  v_dot2c_f32_bf16 v16, s58, v229
  v_dot2c_f32_bf16 v17, v229, v229
  v_dot2c_f32_bf16 v16, s58, v230
  v_dot2c_f32_bf16 v17, v230, v230
  v_dot2c_f32_bf16 v16, s58, v231
  v_dot2c_f32_bf16 v17, v231, v231
  v_dot2c_f32_bf16 v16, s58, v232
  v_dot2c_f32_bf16 v17, v232, v232
  v_dot2c_f32_bf16 v16, s58, v233
  v_dot2c_f32_bf16 v17, v233, v233
  v_dot2c_f32_bf16 v16, s58, v234
  v_dot2c_f32_bf16 v17, v234, v234
  v_dot2c_f32_bf16 v16, s58, v235
  v_dot2c_f32_bf16 v17, v235, v235
  v_dot2c_f32_bf16 v16, s58, v236
  v_dot2c_f32_bf16 v17, v236, v236
  v_dot2c_f32_bf16 v16, s58, v237
  v_dot2c_f32_bf16 v17, v237, v237
  v_dot2c_f32_bf16 v16, s58, v238
  v_dot2c_f32_bf16 v17, v238, v238
  v_dot2c_f32_bf16 v16, s58, v239
  v_dot2c_f32_bf16 v17, v239, v239
  s_nop 1
  v_mov_b32 v18, v16
  s_nop 1
  v_permlane32_swap_b32 v16, v18
  v_add_f32 v16, v16, v18
  v_mov_b32 v19, v17
  s_nop 1
  v_permlane32_swap_b32 v17, v19
  v_add_f32 v17, v17, v19
  v_mul_f32 v16, 0x3b124925, v16
  v_mul_f32 v17, 0x3b124925, v17
  v_fma_f32 v17, -v16, v16, v17
  v_maximum3_f32 v17, v17, 0, 0
  v_cvt_pk_bf16_f32 v17, v17, v17
  v_and_b32 v17, 0xffff0000, v17
  v_add_f32 v17, 0x3727c5ac, v17
  v_cvt_pk_bf16_f32 v17, v17, v17
  v_and_b32 v17, 0xffff0000, v17
  v_sqrt_f32 v17, v17
  s_nop 0
  v_cvt_pk_bf16_f32 v17, v17, v17
  v_and_b32 v17, 0xffff0000, v17
  v_rcp_f32 v112, v17
  v_cvt_pk_bf16_f32 v108, -v16, -v16
  v_cvt_pk_bf16_f32 v109, v17, v17
  v_mul_f32 v113, 0x3fb8aa3b, v112
  v_mov_b32 v110, v108
  v_mov_b32 v111, v109
  v_add_u32 v121, 45056, v1
  v_bfe_u32 v8, v0, 2, 1
  v_bfe_u32 v9, v0, 3, 1
  v_lshlrev_b32 v9, 1, v9
  v_lshl_or_b32 v8, v8, 2, v9
  v_bfe_u32 v9, v0, 1, 1
  v_or_b32 v8, v8, v9
  v_lshrrev_b32 v9, 5, v0
  v_xor_b32 v8, v8, v9
  v_lshlrev_b32 v8, 3, v8
  v_lshl_or_b32 v8, v3, 4, v8
  s_lshl_b32 s57, s49, 11
  v_add_u32 v116, s57, v8
  v_xor_b32 v117, 16, v116
  v_xor_b32 v118, 32, v116
  v_xor_b32 v119, 48, v116
  s_lshl_b32 s57, s49, 9
  s_add_u32 s57, s57, 158720
  v_lshl_add_u32 v114, v3, 2, s57
  v_add_u32 v120, 114688, v1
  v_mov_b32 v8, v108
  v_mov_b32 v9, v109
  v_mov_b32 v10, v112
  ds_write_b128 v114, v[8:11]
  s_min_u32 s57, s56, 27
  s_mul_i32 s58, s57, 37
  s_lshr_b32 s58, s58, 8
  s_mul_i32 s53, s58, 7
  s_sub_u32 s53, s57, s53
  s_lshr_b32 s54, s55, 1
  s_lshl2_add_u32 s54, s54, s58
  s_and_b32 s55, s55, 1
  s_waitcnt lgkmcnt(0)
  s_barrier
  ds_read_b128 v[40:43], v120
  ds_read_b128 v[44:47], v120 offset:1024
  ds_read_b128 v[48:51], v120 offset:2048
  ds_read_b128 v[52:55], v120 offset:3072
  s_waitcnt lgkmcnt(3)
  v_mfma_f32_32x32x16_bf16 v[8:23], v[40:43], v[128:131], 0
  ds_read_b128 v[56:59], v120 offset:4096
  s_waitcnt lgkmcnt(3)
  v_mfma_f32_32x32x16_bf16 v[8:23], v[44:47], v[132:135], v[8:23]
  ds_read_b128 v[60:63], v120 offset:5120
  s_waitcnt lgkmcnt(3)
  v_mfma_f32_32x32x16_bf16 v[8:23], v[48:51], v[136:139], v[8:23]
  ds_read_b128 v[40:43], v120 offset:6144
  s_waitcnt lgkmcnt(3)
  v_mfma_f32_32x32x16_bf16 v[8:23], v[52:55], v[140:143], v[8:23]
  ds_read_b128 v[44:47], v120 offset:7168
  s_waitcnt lgkmcnt(3)
  v_mfma_f32_32x32x16_bf16 v[8:23], v[56:59], v[144:147], v[8:23]
  ds_read_b128 v[48:51], v120 offset:8192
  s_waitcnt lgkmcnt(3)
  v_mfma_f32_32x32x16_bf16 v[8:23], v[60:63], v[148:151], v[8:23]
  ds_read_b128 v[52:55], v120 offset:9216
  s_waitcnt lgkmcnt(3)
  v_mfma_f32_32x32x16_bf16 v[8:23], v[40:43], v[152:155], v[8:23]
  ds_read_b128 v[56:59], v120 offset:10240
  s_waitcnt lgkmcnt(3)
  v_mfma_f32_32x32x16_bf16 v[8:23], v[44:47], v[156:159], v[8:23]
  ds_read_b128 v[60:63], v120 offset:11264
  s_waitcnt lgkmcnt(3)
  v_mfma_f32_32x32x16_bf16 v[8:23], v[48:51], v[160:163], v[8:23]
  s_barrier
  ds_read_b128 v[40:43], v120 offset:12288
  s_waitcnt lgkmcnt(3)
  v_mfma_f32_32x32x16_bf16 v[8:23], v[52:55], v[164:167], v[8:23]
  ds_read_b128 v[44:47], v120 offset:13312
  s_waitcnt lgkmcnt(3)
  v_mfma_f32_32x32x16_bf16 v[8:23], v[56:59], v[168:171], v[8:23]
  ds_read_b128 v[48:51], v120 offset:14336
  s_waitcnt lgkmcnt(3)
  v_mfma_f32_32x32x16_bf16 v[8:23], v[60:63], v[172:175], v[8:23]
  ds_read_b128 v[52:55], v120 offset:15360
  s_waitcnt lgkmcnt(3)
  v_mfma_f32_32x32x16_bf16 v[8:23], v[40:43], v[176:179], v[8:23]
  ds_read_b128 v[56:59], v120 offset:16384
  s_waitcnt lgkmcnt(3)
  v_mfma_f32_32x32x16_bf16 v[8:23], v[44:47], v[180:183], v[8:23]
  ds_read_b128 v[60:63], v120 offset:17408
  s_waitcnt lgkmcnt(3)
  v_mfma_f32_32x32x16_bf16 v[8:23], v[48:51], v[184:187], v[8:23]
  ds_read_b128 v[40:43], v120 offset:18432
  s_waitcnt lgkmcnt(3)
  v_mfma_f32_32x32x16_bf16 v[8:23], v[52:55], v[188:191], v[8:23]
  ds_read_b128 v[44:47], v120 offset:19456
  s_waitcnt lgkmcnt(3)
  v_mfma_f32_32x32x16_bf16 v[8:23], v[56:59], v[192:195], v[8:23]
  ds_read_b128 v[48:51], v120 offset:20480
  s_waitcnt lgkmcnt(3)
  v_mfma_f32_32x32x16_bf16 v[8:23], v[60:63], v[196:199], v[8:23]
  ds_read_b128 v[52:55], v120 offset:21504
  s_waitcnt lgkmcnt(3)
  v_mfma_f32_32x32x16_bf16 v[8:23], v[40:43], v[200:203], v[8:23]
  ds_read_b128 v[56:59], v120 offset:22528
  s_waitcnt lgkmcnt(3)
  v_mfma_f32_32x32x16_bf16 v[8:23], v[44:47], v[204:207], v[8:23]
  ds_read_b128 v[60:63], v120 offset:23552
  s_waitcnt lgkmcnt(3)
  v_mfma_f32_32x32x16_bf16 v[8:23], v[48:51], v[208:211], v[8:23]
  ds_read_b128 v[40:43], v120 offset:24576
  s_waitcnt lgkmcnt(3)
  v_mfma_f32_32x32x16_bf16 v[8:23], v[52:55], v[212:215], v[8:23]
  ds_read_b128 v[44:47], v120 offset:25600
  s_waitcnt lgkmcnt(3)
  v_mfma_f32_32x32x16_bf16 v[8:23], v[56:59], v[216:219], v[8:23]
  ds_read_b128 v[48:51], v120 offset:26624
  s_waitcnt lgkmcnt(3)
  v_mfma_f32_32x32x16_bf16 v[8:23], v[60:63], v[220:223], v[8:23]
  ds_read_b128 v[52:55], v120 offset:27648
  s_waitcnt lgkmcnt(3)
  v_mfma_f32_32x32x16_bf16 v[8:23], v[40:43], v[224:227], v[8:23]
  s_barrier
  ds_read_b128 v[56:59], v120 offset:40960
  s_waitcnt lgkmcnt(3)
  v_mfma_f32_32x32x16_bf16 v[8:23], v[44:47], v[228:231], v[8:23]
  s_waitcnt lgkmcnt(2)
  v_mfma_f32_32x32x16_bf16 v[8:23], v[48:51], v[232:235], v[8:23]
  s_waitcnt lgkmcnt(1)
  v_mfma_f32_32x32x16_bf16 v[8:23], v[52:55], v[236:239], v[8:23]
  s_waitcnt lgkmcnt(0)
  v_mfma_f32_32x32x16_bf16 v[8:23], v[56:59], v[108:111], v[8:23]
  s_barrier
  s_nop 10
  v_maximum3_f32 v122, v8, v9, v10
  v_minimum3_f32 v123, v8, v9, v10
  v_maximum3_f32 v122, v122, v11, v12
  v_minimum3_f32 v123, v123, v11, v12
  v_maximum3_f32 v122, v122, v13, v14
  v_minimum3_f32 v123, v123, v13, v14
  v_maximum3_f32 v122, v122, v15, v16
  v_minimum3_f32 v123, v123, v15, v16
  v_maximum3_f32 v122, v122, v17, v18
  v_minimum3_f32 v123, v123, v17, v18
  v_maximum3_f32 v122, v122, v19, v20
  v_minimum3_f32 v123, v123, v19, v20
  v_maximum3_f32 v124, v122, v21, v21
  v_minimum3_f32 v125, v123, v21, v21
  v_maximum3_f32 v122, v122, v21, v21
  v_minimum3_f32 v123, v123, v21, v21
  v_mov_b32 v99, 0
  v_permlane32_swap_b32 v122, v124
  v_permlane32_swap_b32 v123, v125
  v_maximum3_f32 v122, v122, v124, v124
  v_minimum3_f32 v123, v123, v125, v125
  v_mul_f32 v122, v122, v113
  v_mul_f32 v123, v123, v113
  v_fma_f32 v64, v8, v113, -v122
  v_fma_f32 v78, -v8, v113, v123
  v_fma_f32 v65, v9, v113, -v122
  v_fma_f32 v79, -v9, v113, v123
  v_fma_f32 v66, v10, v113, -v122
  v_fma_f32 v80, -v10, v113, v123
  v_fma_f32 v67, v11, v113, -v122
  v_fma_f32 v81, -v11, v113, v123
  v_fma_f32 v68, v12, v113, -v122
  v_fma_f32 v82, -v12, v113, v123
  v_fma_f32 v69, v13, v113, -v122
  v_fma_f32 v83, -v13, v113, v123
  v_fma_f32 v70, v14, v113, -v122
  v_fma_f32 v84, -v14, v113, v123
  s_barrier
  v_fma_f32 v71, v15, v113, -v122
  v_fma_f32 v85, -v15, v113, v123
  v_fma_f32 v72, v16, v113, -v122
  v_fma_f32 v86, -v16, v113, v123
  v_fma_f32 v73, v17, v113, -v122
  v_fma_f32 v87, -v17, v113, v123
  v_fma_f32 v74, v18, v113, -v122
  v_fma_f32 v88, -v18, v113, v123
  v_fma_f32 v75, v19, v113, -v122
  v_fma_f32 v89, -v19, v113, v123
  v_fma_f32 v76, v20, v113, -v122
  v_fma_f32 v90, -v20, v113, v123
  v_fma_f32 v77, v21, v113, -v122
  v_fma_f32 v91, -v21, v113, v123
  v_exp_f32 v64, v64
  v_exp_f32 v78, v78
  v_exp_f32 v65, v65
  v_exp_f32 v79, v79
  v_exp_f32 v66, v66
  v_exp_f32 v80, v80
  v_add_f32 v122, v64, v65
  v_add_f32 v123, v78, v79
  v_exp_f32 v67, v67
  v_exp_f32 v81, v81
  v_add_f32 v122, v122, v66
  v_add_f32 v123, v123, v80
  v_exp_f32 v68, v68
  v_exp_f32 v82, v82
  v_add_f32 v122, v122, v67
  v_add_f32 v123, v123, v81
  v_exp_f32 v69, v69
  v_exp_f32 v83, v83
  v_add_f32 v122, v122, v68
  v_add_f32 v123, v123, v82
  v_exp_f32 v70, v70
  v_exp_f32 v84, v84
  v_add_f32 v122, v122, v69
  v_add_f32 v123, v123, v83
  v_exp_f32 v71, v71
  v_exp_f32 v85, v85
  v_add_f32 v122, v122, v70
  v_add_f32 v123, v123, v84
  v_exp_f32 v72, v72
  v_exp_f32 v86, v86
  v_add_f32 v122, v122, v71
  v_add_f32 v123, v123, v85
  v_exp_f32 v73, v73
  v_exp_f32 v87, v87
  v_add_f32 v122, v122, v72
  v_add_f32 v123, v123, v86
  v_exp_f32 v74, v74
  v_exp_f32 v88, v88
  v_add_f32 v122, v122, v73
  v_add_f32 v123, v123, v87
  v_exp_f32 v75, v75
  v_exp_f32 v89, v89
  v_add_f32 v122, v122, v74
  v_add_f32 v123, v123, v88
  v_exp_f32 v76, v76
  v_exp_f32 v90, v90
  v_add_f32 v122, v122, v75
  v_add_f32 v123, v123, v89
  v_exp_f32 v77, v77
  v_exp_f32 v91, v91
  v_add_f32 v122, v122, v76
  v_add_f32 v123, v123, v90
  v_add_f32 v124, v122, v77
  v_add_f32 v122, v122, v77
  v_add_f32 v125, v123, v91
  v_add_f32 v123, v123, v91
  v_mov_b32 v107, 0
  v_permlane32_swap_b32 v122, v124
  v_permlane32_swap_b32 v123, v125
  v_add_f32 v122, v122, v124
  v_add_f32 v123, v123, v125
  v_rcp_f32 v122, v122
  v_rcp_f32 v123, v123
  v_mul_f32 v64, v64, v122
  v_mul_f32 v65, v65, v122
  v_mul_f32 v66, v66, v122
  v_mul_f32 v67, v67, v122
  v_mul_f32 v68, v68, v122
  v_mul_f32 v69, v69, v122
  v_mul_f32 v70, v70, v122
  v_mul_f32 v71, v71, v122
  v_mul_f32 v72, v72, v122
  v_mul_f32 v73, v73, v122
  v_mul_f32 v74, v74, v122
  v_mul_f32 v75, v75, v122
  v_mul_f32 v76, v76, v122
  v_mul_f32 v77, v77, v122
  v_cvt_pk_bf16_f32 v92, v64, v65
  v_cvt_pk_bf16_f32 v93, v66, v67
  v_cvt_pk_bf16_f32 v94, v68, v69
  v_cvt_pk_bf16_f32 v95, v70, v71
  v_cvt_pk_bf16_f32 v96, v72, v73
  v_cvt_pk_bf16_f32 v97, v74, v75
  v_cvt_pk_bf16_f32 v98, v76, v77
  ds_write_b64 v116, v[92:93]
  ds_write_b64 v117, v[94:95]
  ds_write_b64 v118, v[96:97]
  ds_write_b64 v119, v[98:99]
  v_mul_f32 v78, v78, v123
  v_mul_f32 v79, v79, v123
  v_mul_f32 v80, v80, v123
  v_mul_f32 v81, v81, v123
  v_mul_f32 v82, v82, v123
  v_mul_f32 v83, v83, v123
  v_mul_f32 v84, v84, v123
  v_mul_f32 v85, v85, v123
  v_mul_f32 v86, v86, v123
  v_mul_f32 v87, v87, v123
  v_mul_f32 v88, v88, v123
  v_mul_f32 v89, v89, v123
  v_mul_f32 v90, v90, v123
  v_mul_f32 v91, v91, v123
  v_cvt_pk_bf16_f32 v100, v78, v79
  v_cvt_pk_bf16_f32 v101, v80, v81
  v_cvt_pk_bf16_f32 v102, v82, v83
  v_cvt_pk_bf16_f32 v103, v84, v85
  v_cvt_pk_bf16_f32 v104, v86, v87
  v_cvt_pk_bf16_f32 v105, v88, v89
  v_cvt_pk_bf16_f32 v106, v90, v91
  ds_write_b64 v116, v[100:101] offset:8192
  ds_write_b64 v117, v[102:103] offset:8192
  ds_write_b64 v118, v[104:105] offset:8192
  ds_write_b64 v119, v[106:107] offset:8192
  s_waitcnt lgkmcnt(0)
  s_barrier
  ds_read_b128 v[248:251], v120 offset:43008
  ds_read_b128 v[40:43], v121
  ds_read_b128 v[44:47], v121 offset:1024
  ds_read_b128 v[48:51], v121 offset:2048
  ds_read_b128 v[52:55], v121 offset:3072
  s_waitcnt lgkmcnt(3)
  v_mfma_f32_32x32x16_bf16 v[8:23], v[40:43], v[128:131], 0
  ds_read_b128 v[56:59], v121 offset:4096
  s_waitcnt lgkmcnt(3)
  v_mfma_f32_32x32x16_bf16 v[8:23], v[44:47], v[132:135], v[8:23]
  ds_read_b128 v[60:63], v121 offset:5120
  s_waitcnt lgkmcnt(3)
  v_mfma_f32_32x32x16_bf16 v[8:23], v[48:51], v[136:139], v[8:23]
  ds_read_b128 v[40:43], v121 offset:6144
  s_waitcnt lgkmcnt(3)
  v_mfma_f32_32x32x16_bf16 v[8:23], v[52:55], v[140:143], v[8:23]
  ds_read_b128 v[44:47], v121 offset:7168
  s_waitcnt lgkmcnt(3)
  v_mfma_f32_32x32x16_bf16 v[8:23], v[56:59], v[144:147], v[8:23]
  ds_read_b128 v[48:51], v121 offset:8192
  s_waitcnt lgkmcnt(3)
  v_mfma_f32_32x32x16_bf16 v[8:23], v[60:63], v[148:151], v[8:23]
  ds_read_b128 v[52:55], v121 offset:9216
  s_waitcnt lgkmcnt(3)
  v_mfma_f32_32x32x16_bf16 v[8:23], v[40:43], v[152:155], v[8:23]
  ds_read_b128 v[56:59], v121 offset:10240
  s_waitcnt lgkmcnt(3)
  v_mfma_f32_32x32x16_bf16 v[8:23], v[44:47], v[156:159], v[8:23]
  ds_read_b128 v[60:63], v121 offset:11264
  s_waitcnt lgkmcnt(3)
  v_mfma_f32_32x32x16_bf16 v[8:23], v[48:51], v[160:163], v[8:23]
  ds_read_b128 v[40:43], v121 offset:12288
  s_waitcnt lgkmcnt(3)
  v_mfma_f32_32x32x16_bf16 v[8:23], v[52:55], v[164:167], v[8:23]
  ds_read_b128 v[44:47], v121 offset:13312
  s_waitcnt lgkmcnt(3)
  v_mfma_f32_32x32x16_bf16 v[8:23], v[56:59], v[168:171], v[8:23]
  ds_read_b128 v[48:51], v121 offset:14336
  s_waitcnt lgkmcnt(3)
  v_mfma_f32_32x32x16_bf16 v[8:23], v[60:63], v[172:175], v[8:23]
  ds_read_b128 v[52:55], v121 offset:15360
  s_waitcnt lgkmcnt(3)
  v_mfma_f32_32x32x16_bf16 v[8:23], v[40:43], v[176:179], v[8:23]
  ds_read_b128 v[56:59], v121 offset:16384
  s_waitcnt lgkmcnt(3)
  v_mfma_f32_32x32x16_bf16 v[8:23], v[44:47], v[180:183], v[8:23]
  ds_read_b128 v[60:63], v121 offset:17408
  s_waitcnt lgkmcnt(3)
  v_mfma_f32_32x32x16_bf16 v[8:23], v[48:51], v[184:187], v[8:23]
  ds_read_b128 v[40:43], v121 offset:18432
  s_waitcnt lgkmcnt(3)
  v_mfma_f32_32x32x16_bf16 v[8:23], v[52:55], v[188:191], v[8:23]
  ds_read_b128 v[44:47], v121 offset:19456
  s_waitcnt lgkmcnt(3)
  v_mfma_f32_32x32x16_bf16 v[8:23], v[56:59], v[192:195], v[8:23]
  ds_read_b128 v[48:51], v121 offset:20480
  s_waitcnt lgkmcnt(3)
  v_mfma_f32_32x32x16_bf16 v[8:23], v[60:63], v[196:199], v[8:23]
  ds_read_b128 v[52:55], v121 offset:21504
  s_waitcnt lgkmcnt(3)
  v_mfma_f32_32x32x16_bf16 v[8:23], v[40:43], v[200:203], v[8:23]
  ds_read_b128 v[56:59], v121 offset:22528
  s_waitcnt lgkmcnt(3)
  v_mfma_f32_32x32x16_bf16 v[8:23], v[44:47], v[204:207], v[8:23]
  ds_read_b128 v[60:63], v121 offset:23552
  s_waitcnt lgkmcnt(3)
  v_mfma_f32_32x32x16_bf16 v[8:23], v[48:51], v[208:211], v[8:23]
  ds_read_b128 v[40:43], v121 offset:24576
  s_waitcnt lgkmcnt(3)
  v_mfma_f32_32x32x16_bf16 v[8:23], v[52:55], v[212:215], v[8:23]
  ds_read_b128 v[44:47], v121 offset:25600
  s_waitcnt lgkmcnt(3)
  v_mfma_f32_32x32x16_bf16 v[8:23], v[56:59], v[216:219], v[8:23]
  ds_read_b128 v[48:51], v121 offset:26624
  s_waitcnt lgkmcnt(3)
  v_mfma_f32_32x32x16_bf16 v[8:23], v[60:63], v[220:223], v[8:23]
  ds_read_b128 v[52:55], v121 offset:27648
  s_waitcnt lgkmcnt(3)
  v_mfma_f32_32x32x16_bf16 v[8:23], v[40:43], v[224:227], v[8:23]
  s_waitcnt lgkmcnt(2)
  v_mfma_f32_32x32x16_bf16 v[8:23], v[44:47], v[228:231], v[8:23]
  s_waitcnt lgkmcnt(1)
  v_mfma_f32_32x32x16_bf16 v[8:23], v[48:51], v[232:235], v[8:23]
  s_waitcnt lgkmcnt(0)
  v_mfma_f32_32x32x16_bf16 v[8:23], v[52:55], v[236:239], v[8:23]
  v_mfma_f32_32x32x16_bf16 v[8:23], v[248:251], v[108:111], v[8:23]
  s_nop 11
  v_maximum3_f32 v122, v8, v9, v10
  v_minimum3_f32 v123, v8, v9, v10
  v_maximum3_f32 v122, v122, v11, v12
  v_minimum3_f32 v123, v123, v11, v12
  v_maximum3_f32 v122, v122, v13, v14
  v_minimum3_f32 v123, v123, v13, v14
  v_maximum3_f32 v122, v122, v15, v16
  v_minimum3_f32 v123, v123, v15, v16
  v_maximum3_f32 v122, v122, v17, v18
  v_minimum3_f32 v123, v123, v17, v18
  v_maximum3_f32 v122, v122, v19, v20
  v_minimum3_f32 v123, v123, v19, v20
  v_maximum3_f32 v124, v122, v21, v21
  v_minimum3_f32 v125, v123, v21, v21
  v_maximum3_f32 v122, v122, v21, v21
  v_minimum3_f32 v123, v123, v21, v21
  v_mov_b32 v99, 0
  v_permlane32_swap_b32 v122, v124
  v_permlane32_swap_b32 v123, v125
  v_maximum3_f32 v122, v122, v124, v124
  v_minimum3_f32 v123, v123, v125, v125
  v_mul_f32 v122, v122, v113
  v_mul_f32 v123, v123, v113
  v_fma_f32 v64, v8, v113, -v122
  v_fma_f32 v78, -v8, v113, v123
  v_fma_f32 v65, v9, v113, -v122
  v_fma_f32 v79, -v9, v113, v123
  v_fma_f32 v66, v10, v113, -v122
  v_fma_f32 v80, -v10, v113, v123
  v_fma_f32 v67, v11, v113, -v122
  v_fma_f32 v81, -v11, v113, v123
  v_fma_f32 v68, v12, v113, -v122
  v_fma_f32 v82, -v12, v113, v123
  v_fma_f32 v69, v13, v113, -v122
  v_fma_f32 v83, -v13, v113, v123
  v_fma_f32 v70, v14, v113, -v122
  v_fma_f32 v84, -v14, v113, v123
  v_fma_f32 v71, v15, v113, -v122
  v_fma_f32 v85, -v15, v113, v123
  v_fma_f32 v72, v16, v113, -v122
  v_fma_f32 v86, -v16, v113, v123
  v_fma_f32 v73, v17, v113, -v122
  v_fma_f32 v87, -v17, v113, v123
  v_fma_f32 v74, v18, v113, -v122
  v_fma_f32 v88, -v18, v113, v123
  v_fma_f32 v75, v19, v113, -v122
  v_fma_f32 v89, -v19, v113, v123
  v_fma_f32 v76, v20, v113, -v122
  v_fma_f32 v90, -v20, v113, v123
  v_fma_f32 v77, v21, v113, -v122
  v_fma_f32 v91, -v21, v113, v123
  v_exp_f32 v64, v64
  v_exp_f32 v78, v78
  v_exp_f32 v65, v65
  v_exp_f32 v79, v79
  v_exp_f32 v66, v66
  v_exp_f32 v80, v80
  v_add_f32 v122, v64, v65
  v_add_f32 v123, v78, v79
  v_exp_f32 v67, v67
  v_exp_f32 v81, v81
  v_add_f32 v122, v122, v66
  v_add_f32 v123, v123, v80
  v_exp_f32 v68, v68
  v_exp_f32 v82, v82
  v_add_f32 v122, v122, v67
  v_add_f32 v123, v123, v81
  v_exp_f32 v69, v69
  v_exp_f32 v83, v83
  v_add_f32 v122, v122, v68
  v_add_f32 v123, v123, v82
  v_exp_f32 v70, v70
  v_exp_f32 v84, v84
  v_add_f32 v122, v122, v69
  v_add_f32 v123, v123, v83
  v_exp_f32 v71, v71
  v_exp_f32 v85, v85
  v_add_f32 v122, v122, v70
  v_add_f32 v123, v123, v84
  v_exp_f32 v72, v72
  v_exp_f32 v86, v86
  v_add_f32 v122, v122, v71
  v_add_f32 v123, v123, v85
  v_exp_f32 v73, v73
  v_exp_f32 v87, v87
  v_add_f32 v122, v122, v72
  v_add_f32 v123, v123, v86
  v_exp_f32 v74, v74
  v_exp_f32 v88, v88
  v_add_f32 v122, v122, v73
  v_add_f32 v123, v123, v87
  v_exp_f32 v75, v75
  v_exp_f32 v89, v89
  v_add_f32 v122, v122, v74
  v_add_f32 v123, v123, v88
  v_exp_f32 v76, v76
  v_exp_f32 v90, v90
  v_add_f32 v122, v122, v75
  v_add_f32 v123, v123, v89
  v_exp_f32 v77, v77
  v_exp_f32 v91, v91
  v_add_f32 v122, v122, v76
  v_add_f32 v123, v123, v90
  v_add_f32 v124, v122, v77
  v_add_f32 v122, v122, v77
  v_add_f32 v125, v123, v91
  v_add_f32 v123, v123, v91
  v_mov_b32 v107, 0
  v_permlane32_swap_b32 v122, v124
  v_permlane32_swap_b32 v123, v125
  v_add_f32 v122, v122, v124
  v_add_f32 v123, v123, v125
  v_rcp_f32 v122, v122
  v_rcp_f32 v123, v123
  s_nop 0
  v_pk_mul_f32 v[64:65], v[64:65], v[122:123] op_sel_hi:[1,0]
  v_pk_mul_f32 v[66:67], v[66:67], v[122:123] op_sel_hi:[1,0]
  v_pk_mul_f32 v[68:69], v[68:69], v[122:123] op_sel_hi:[1,0]
  v_pk_mul_f32 v[70:71], v[70:71], v[122:123] op_sel_hi:[1,0]
  v_pk_mul_f32 v[72:73], v[72:73], v[122:123] op_sel_hi:[1,0]
  v_pk_mul_f32 v[74:75], v[74:75], v[122:123] op_sel_hi:[1,0]
  v_pk_mul_f32 v[76:77], v[76:77], v[122:123] op_sel_hi:[1,0]
  v_cvt_pk_bf16_f32 v92, v64, v65
  v_cvt_pk_bf16_f32 v93, v66, v67
  v_cvt_pk_bf16_f32 v94, v68, v69
  v_cvt_pk_bf16_f32 v95, v70, v71
  v_cvt_pk_bf16_f32 v96, v72, v73
  v_cvt_pk_bf16_f32 v97, v74, v75
  v_cvt_pk_bf16_f32 v98, v76, v77
  v_pk_mul_f32 v[78:79], v[78:79], v[122:123] op_sel:[0,1]
  v_pk_mul_f32 v[80:81], v[80:81], v[122:123] op_sel:[0,1]
  v_pk_mul_f32 v[82:83], v[82:83], v[122:123] op_sel:[0,1]
  v_pk_mul_f32 v[84:85], v[84:85], v[122:123] op_sel:[0,1]
  v_pk_mul_f32 v[86:87], v[86:87], v[122:123] op_sel:[0,1]
  v_pk_mul_f32 v[88:89], v[88:89], v[122:123] op_sel:[0,1]
  v_pk_mul_f32 v[90:91], v[90:91], v[122:123] op_sel:[0,1]
  v_cvt_pk_bf16_f32 v100, v78, v79
  v_cvt_pk_bf16_f32 v101, v80, v81
  v_cvt_pk_bf16_f32 v102, v82, v83
  v_cvt_pk_bf16_f32 v103, v84, v85
  v_cvt_pk_bf16_f32 v104, v86, v87
  v_cvt_pk_bf16_f32 v105, v88, v89
  v_cvt_pk_bf16_f32 v106, v90, v91
.Lh1:
  v_lshrrev_b32 v6, 5, v0
  v_lshlrev_b32 v5, 2, v3
  v_lshl_or_b32 v5, v6, 12, v5
  s_lshl_b32 s57, s52, 7
  s_add_u32 s57, s57, s48
  s_lshl_b32 s1, s57, 9
  s_add_u32 s1, s1, 4444160
  v_and_b32 v7, 15, v0
  v_lshl_or_b32 v7, v6, 4, v7
  v_bfe_u32 v6, v0, 4, 1
  v_lshl_or_b32 v7, v6, 5, v7
  s_lshr_b32 s57, s48, 1
  s_lshl_b32 s57, s57, 7
  s_and_b32 s58, s48, 1
  s_add_u32 s57, s57, s58
  s_lshl_b32 s57, s57, 3
  s_add_u32 s57, s57, 24576
  v_lshl_add_u32 v7, v7, 4, s57
  s_movk_i32 s58, 0x3f80
  s_mov_b32 s0, 0x3f800000
  s_and_b32 s57, s51, 3
  s_lshl_b32 s57, s57, 14
  s_xor_b32 s12, s57, 0
  s_add_u32 s12, s12, s1
  s_xor_b32 s13, s57, 8192
  s_add_u32 s13, s13, s1
  s_xor_b32 s14, s57, 16384
  s_add_u32 s14, s14, s1
  s_xor_b32 s15, s57, 24576
  s_add_u32 s15, s15, s1
  s_xor_b32 s16, s57, 32768
  s_add_u32 s16, s16, s1
  s_xor_b32 s17, s57, 40960
  s_add_u32 s17, s17, s1
  s_xor_b32 s18, s57, 49152
  s_add_u32 s18, s18, s1
  s_xor_b32 s19, s57, 57344
  s_add_u32 s19, s19, s1
  s_mul_i32 s20, s55, 7
  s_add_u32 s20, s20, s53
  s_lshl3_add_u32 s20, s20, s48
  s_mul_i32 s21, s20, 28672
  s_add_u32 s21, s21, 9424896
  s_lshl_b32 s22, s20, 5
  s_add_u32 s22, s22, 4096
  v_min_u32 v56, 24, v3
  s_cmp_lg_u32 s48, 7
  s_cbranch_scc1 .Lh1go
  v_and_b32 v12, 15, v0
  v_lshlrev_b32 v12, 7, v12
  s_lshl_b32 s1, s52, 11
  s_add_u32 s1, s1, 143360
  s_cmp_lt_u32 s56, 28
  s_cbranch_scc1 .Lh1poll
  s_sleep 16
.Lh1poll:
  s_movk_i32 s59, 0x7fff
.Lpoll_22:
  buffer_load_dword v8, v12, s[4:7], s1 offen sc1
  s_waitcnt vmcnt(0)
  v_cmp_ne_u32 vcc, s50, v8
  s_cbranch_vccz .Ldone_23
  s_sleep 1
  s_sub_u32 s59, s59, 1
  s_cbranch_scc0 .Lpoll_22
.Ldone_23:
.Lh1go:
  s_movk_i32 s59, 0x7fff
  s_waitcnt vmcnt(0) lgkmcnt(0)
  s_barrier
.Lh1again:
  buffer_load_dwordx4 v[24:27], v5, s[4:7], s12 offen sc1
  buffer_load_dwordx4 v[28:31], v5, s[4:7], s13 offen sc1
  buffer_load_dwordx4 v[32:35], v5, s[4:7], s14 offen sc1
  buffer_load_dwordx4 v[36:39], v5, s[4:7], s15 offen sc1
  buffer_load_dwordx4 v[40:43], v5, s[4:7], s16 offen sc1
  buffer_load_dwordx4 v[44:47], v5, s[4:7], s17 offen sc1
  buffer_load_dwordx4 v[48:51], v5, s[4:7], s18 offen sc1
  buffer_load_dwordx4 v[52:55], v5, s[4:7], s19 offen sc1
  buffer_load_dword v57, v56, s[4:7], s22 offen sc1
  v_mov_b32 v8, 0
  v_mov_b32 v9, 0
  v_mov_b32 v10, 0
  v_mov_b32 v11, 0
  v_mov_b32 v12, 0
  v_mov_b32 v13, 0
  v_mov_b32 v14, 0
  v_mov_b32 v15, 0
  s_waitcnt vmcnt(8)
  v_dot2c_f32_bf16 v8, s58, v24
  v_dot2c_f32_bf16 v9, s0, v24
  v_dot2c_f32_bf16 v10, s58, v25
  v_dot2c_f32_bf16 v11, s0, v25
  v_dot2c_f32_bf16 v12, s58, v26
  v_dot2c_f32_bf16 v13, s0, v26
  v_dot2c_f32_bf16 v14, s58, v27
  v_dot2c_f32_bf16 v15, s0, v27
  s_waitcnt vmcnt(7)
  v_dot2c_f32_bf16 v8, s58, v28
  v_dot2c_f32_bf16 v9, s0, v28
  v_dot2c_f32_bf16 v10, s58, v29
  v_dot2c_f32_bf16 v11, s0, v29
  v_dot2c_f32_bf16 v12, s58, v30
  v_dot2c_f32_bf16 v13, s0, v30
  v_dot2c_f32_bf16 v14, s58, v31
  v_dot2c_f32_bf16 v15, s0, v31
  s_waitcnt vmcnt(6)
  v_dot2c_f32_bf16 v8, s58, v32
  v_dot2c_f32_bf16 v9, s0, v32
  v_dot2c_f32_bf16 v10, s58, v33
  v_dot2c_f32_bf16 v11, s0, v33
  v_dot2c_f32_bf16 v12, s58, v34
  v_dot2c_f32_bf16 v13, s0, v34
  v_dot2c_f32_bf16 v14, s58, v35
  v_dot2c_f32_bf16 v15, s0, v35
  s_waitcnt vmcnt(5)
  v_dot2c_f32_bf16 v8, s58, v36
  v_dot2c_f32_bf16 v9, s0, v36
  v_dot2c_f32_bf16 v10, s58, v37
  v_dot2c_f32_bf16 v11, s0, v37
  v_dot2c_f32_bf16 v12, s58, v38
  v_dot2c_f32_bf16 v13, s0, v38
  v_dot2c_f32_bf16 v14, s58, v39
  v_dot2c_f32_bf16 v15, s0, v39
  s_waitcnt vmcnt(4)
  v_dot2c_f32_bf16 v8, s58, v40
  v_dot2c_f32_bf16 v9, s0, v40
  v_dot2c_f32_bf16 v10, s58, v41
  v_dot2c_f32_bf16 v11, s0, v41
  v_dot2c_f32_bf16 v12, s58, v42
  v_dot2c_f32_bf16 v13, s0, v42
  v_dot2c_f32_bf16 v14, s58, v43
  v_dot2c_f32_bf16 v15, s0, v43
  s_waitcnt vmcnt(3)
  v_dot2c_f32_bf16 v8, s58, v44
  v_dot2c_f32_bf16 v9, s0, v44
  v_dot2c_f32_bf16 v10, s58, v45
  v_dot2c_f32_bf16 v11, s0, v45
  v_dot2c_f32_bf16 v12, s58, v46
  v_dot2c_f32_bf16 v13, s0, v46
  v_dot2c_f32_bf16 v14, s58, v47
  v_dot2c_f32_bf16 v15, s0, v47
  s_waitcnt vmcnt(2)
  v_dot2c_f32_bf16 v8, s58, v48
  v_dot2c_f32_bf16 v9, s0, v48
  v_dot2c_f32_bf16 v10, s58, v49
  v_dot2c_f32_bf16 v11, s0, v49
  v_dot2c_f32_bf16 v12, s58, v50
  v_dot2c_f32_bf16 v13, s0, v50
  v_dot2c_f32_bf16 v14, s58, v51
  v_dot2c_f32_bf16 v15, s0, v51
  s_waitcnt vmcnt(1)
  v_dot2c_f32_bf16 v8, s58, v52
  v_dot2c_f32_bf16 v9, s0, v52
  v_dot2c_f32_bf16 v10, s58, v53
  v_dot2c_f32_bf16 v11, s0, v53
  v_dot2c_f32_bf16 v12, s58, v54
  v_dot2c_f32_bf16 v13, s0, v54
  v_dot2c_f32_bf16 v14, s58, v55
  v_dot2c_f32_bf16 v15, s0, v55
  v_permlane32_swap_b32 v8, v12
  v_permlane32_swap_b32 v9, v13
  v_permlane32_swap_b32 v10, v14
  v_permlane32_swap_b32 v11, v15
  v_add_f32 v8, v8, v12
  v_add_f32 v9, v9, v13
  v_add_f32 v10, v10, v14
  v_add_f32 v11, v11, v15
  v_cmp_u_f32 vcc, v8, v10
  v_cvt_pk_bf16_f32 v12, v8, v9
  v_cvt_pk_bf16_f32 v13, v10, v11
  s_cbranch_vccnz .Lh1chk
.Lh1ok:
  ds_write_b64 v7, v[12:13]
  s_waitcnt lgkmcnt(0)
  s_barrier
  s_cmp_ge_u32 s48, 4
  s_cbranch_scc1 .LB2
  ds_read_b128 v[24:27], v1 offset:24576
  ds_read_b128 v[28:31], v1 offset:25600
  ds_read_b128 v[32:35], v1 offset:26624
  ds_read_b128 v[36:39], v1 offset:27648
  v_bfe_u32 v46, v0, 4, 1
  v_lshlrev_b32 v46, 14, v46
  v_lshrrev_b32 v47, 5, v0
  v_lshl_or_b32 v46, v47, 8, v46
  v_and_b32 v47, 15, v0
  v_lshl_or_b32 v46, v47, 4, v46
  s_lshl2_add_u32 s32, s51, s49
  s_lshl_b32 s32, s32, 5
  s_add_u32 s32, s32, s52
  s_lshl_b32 s32, s32, 10
  s_add_u32 s32, s32, 5492736
  s_cmp_eq_u32 s48, 3
  s_cselect_b32 s57, 0, 2146435072
  v_or_b32 v47, s57, v3
  s_lshl4_add_u32 s57, s51, s52
  s_lshl_b32 s57, s57, 7
  s_add_u32 s57, s57, 176128
  s_waitcnt lgkmcnt(3)
  v_mfma_f32_32x32x16_bf16 v[8:23], v[24:27], v[92:95], 0
  s_waitcnt lgkmcnt(2)
  v_mfma_f32_32x32x16_bf16 v[8:23], v[28:31], v[96:99], v[8:23]
  s_waitcnt lgkmcnt(1)
  v_mfma_f32_32x32x16_bf16 v[8:23], v[32:35], v[100:103], v[8:23]
  s_waitcnt lgkmcnt(0)
  v_mfma_f32_32x32x16_bf16 v[8:23], v[36:39], v[104:107], v[8:23]
  v_mov_b32 v31, 0
  s_nop 10
  v_add_f32 v22, 0x358637bd, v22
  v_rcp_f32 v22, v22
  s_nop 0
  v_pk_mul_f32 v[8:9], v[8:9], v[22:23] op_sel_hi:[1,0]
  v_pk_mul_f32 v[10:11], v[10:11], v[22:23] op_sel_hi:[1,0]
  v_pk_mul_f32 v[12:13], v[12:13], v[22:23] op_sel_hi:[1,0]
  v_pk_mul_f32 v[14:15], v[14:15], v[22:23] op_sel_hi:[1,0]
  v_pk_mul_f32 v[16:17], v[16:17], v[22:23] op_sel_hi:[1,0]
  v_pk_mul_f32 v[18:19], v[18:19], v[22:23] op_sel_hi:[1,0]
  v_pk_mul_f32 v[20:21], v[20:21], v[22:23] op_sel_hi:[1,0]
  v_cvt_pk_bf16_f32 v24, v8, v9
  v_cvt_pk_bf16_f32 v25, v10, v11
  v_cvt_pk_bf16_f32 v26, v12, v13
  v_cvt_pk_bf16_f32 v27, v14, v15
  buffer_store_dwordx4 v[24:27], v46, s[4:7], s32 offen sc1
  v_cvt_pk_bf16_f32 v28, v16, v17
  v_cvt_pk_bf16_f32 v29, v18, v19
  v_cvt_pk_bf16_f32 v30, v20, v21
  buffer_store_dwordx4 v[28:31], v46, s[4:7], s32 offen offset:512 sc1
  buffer_store_dword v2, v47, s[4:7], s57 offen sc1
  s_cmp_ge_u32 s56, 28
  s_cbranch_scc1 .Lexit
  s_waitcnt vmcnt(3)
  v_cmp_ne_u32 vcc, s50, v57
  s_cbranch_vccz .Ldone_26
  s_movk_i32 s59, 0x7fff
.Lpoll_25:
  buffer_load_dword v57, v56, s[4:7], s22 offen sc1
  s_waitcnt vmcnt(0)
  v_cmp_ne_u32 vcc, s50, v57
  s_cbranch_vccz .Ldone_26
  s_sleep 1
  s_sub_u32 s59, s59, 1
  s_cbranch_scc0 .Lpoll_25
.Ldone_26:
  s_add_u32 s57, s21, 0
  buffer_load_dwordx4 v[128:131], v1, s[4:7], s57 offen sc1
  buffer_load_dwordx4 v[132:135], v1, s[4:7], s57 offen offset:1024 sc1
  buffer_load_dwordx4 v[136:139], v1, s[4:7], s57 offen offset:2048 sc1
  buffer_load_dwordx4 v[140:143], v1, s[4:7], s57 offen offset:3072 sc1
  s_add_u32 s57, s21, 4096
  buffer_load_dwordx4 v[144:147], v1, s[4:7], s57 offen sc1
  buffer_load_dwordx4 v[148:151], v1, s[4:7], s57 offen offset:1024 sc1
  buffer_load_dwordx4 v[152:155], v1, s[4:7], s57 offen offset:2048 sc1
  buffer_load_dwordx4 v[156:159], v1, s[4:7], s57 offen offset:3072 sc1
  s_add_u32 s57, s21, 8192
  buffer_load_dwordx4 v[160:163], v1, s[4:7], s57 offen sc1
  buffer_load_dwordx4 v[164:167], v1, s[4:7], s57 offen offset:1024 sc1
  buffer_load_dwordx4 v[168:171], v1, s[4:7], s57 offen offset:2048 sc1
  buffer_load_dwordx4 v[172:175], v1, s[4:7], s57 offen offset:3072 sc1
  s_barrier
  s_barrier
  s_add_u32 s57, s21, 12288
  buffer_load_dwordx4 v[176:179], v1, s[4:7], s57 offen sc1
  buffer_load_dwordx4 v[180:183], v1, s[4:7], s57 offen offset:1024 sc1
  buffer_load_dwordx4 v[184:187], v1, s[4:7], s57 offen offset:2048 sc1
  buffer_load_dwordx4 v[188:191], v1, s[4:7], s57 offen offset:3072 sc1
  s_add_u32 s57, s21, 16384
  buffer_load_dwordx4 v[192:195], v1, s[4:7], s57 offen sc1
  buffer_load_dwordx4 v[196:199], v1, s[4:7], s57 offen offset:1024 sc1
  buffer_load_dwordx4 v[200:203], v1, s[4:7], s57 offen offset:2048 sc1
  buffer_load_dwordx4 v[204:207], v1, s[4:7], s57 offen offset:3072 sc1
  s_barrier
  s_add_u32 s57, s21, 20480
  buffer_load_dwordx4 v[208:211], v1, s[4:7], s57 offen sc1
  buffer_load_dwordx4 v[212:215], v1, s[4:7], s57 offen offset:1024 sc1
  buffer_load_dwordx4 v[216:219], v1, s[4:7], s57 offen offset:2048 sc1
  buffer_load_dwordx4 v[220:223], v1, s[4:7], s57 offen offset:3072 sc1
  s_add_u32 s57, s21, 24576
  buffer_load_dwordx4 v[224:227], v1, s[4:7], s57 offen sc1
  buffer_load_dwordx4 v[228:231], v1, s[4:7], s57 offen offset:1024 sc1
  buffer_load_dwordx4 v[232:235], v1, s[4:7], s57 offen offset:2048 sc1
  buffer_load_dwordx4 v[236:239], v1, s[4:7], s57 offen offset:3072 sc1
  s_lshl_b32 s57, s20, 12
  s_add_u32 s57, s57, 2150400
  v_lshrrev_b32 v6, 5, v0
  v_lshlrev_b32 v7, 2, v3
  v_lshl_or_b32 v5, v6, 11, v7
  s_movk_i32 s59, 0x7fff
.Lgpoll_28:
  buffer_load_dwordx4 v[8:11], v5, s[4:7], s57 offen sc1
  buffer_load_dwordx4 v[12:15], v5, s[4:7], s57 offen offset:512 sc1
  buffer_load_dwordx4 v[16:19], v5, s[4:7], s57 offen offset:1024 sc1
  buffer_load_dwordx4 v[20:23], v7, s[4:7], s57 offen offset:1536 sc1
  s_waitcnt vmcnt(3)
  v_cmpx_eq_u32 vcc, s50, v9
  v_cmpx_eq_u32 vcc, s50, v11
  s_waitcnt vmcnt(2)
  v_cmpx_eq_u32 vcc, s50, v13
  v_cmpx_eq_u32 vcc, s50, v15
  s_waitcnt vmcnt(1)
  v_cmpx_eq_u32 vcc, s50, v17
  v_cmpx_eq_u32 vcc, s50, v19
  s_waitcnt vmcnt(0)
  v_cmpx_eq_u32 vcc, s50, v21
  v_cmpx_eq_u32 vcc, s50, v23
  s_cmp_eq_u64 exec, -1
  s_mov_b64 exec, -1
  s_cbranch_scc1 .Lgdone_29
  s_sleep 1
  s_sub_u32 s59, s59, 1
  s_cbranch_scc0 .Lgpoll_28
.Lgdone_29:
  v_cmp_gt_u32 vcc, 32, v0
  v_add_f32 v10, v10, v14
  v_add_f32 v10, v10, v18
  v_cndmask_b32 v22, 0, v22, vcc
  v_add_f32 v10, v10, v22
  v_mov_b32 v11, v10
  s_nop 1
  v_permlane32_swap_b32 v10, v11
  v_add_f32 v10, v10, v11
  v_cndmask_b32 v20, v10, v20, vcc
  v_cvt_pk_bf16_f32 v9, v8, v8
  v_and_b32 v9, 0xffff0000, v9
  v_sub_f32 v240, v8, v9
  v_cvt_pk_bf16_f32 v240, v9, v240
  v_cvt_pk_bf16_f32 v13, v12, v12
  v_and_b32 v13, 0xffff0000, v13
  v_sub_f32 v241, v12, v13
  v_cvt_pk_bf16_f32 v241, v13, v241
  v_cvt_pk_bf16_f32 v17, v16, v16
  v_and_b32 v17, 0xffff0000, v17
  v_sub_f32 v242, v16, v17
  v_cvt_pk_bf16_f32 v242, v17, v242
  v_cvt_pk_bf16_f32 v21, v20, v20
  v_and_b32 v21, 0xffff0000, v21
  v_sub_f32 v243, v20, v21
  v_cvt_pk_bf16_f32 v243, v21, v243
  s_cmp_ge_u32 s53, 4
  s_addc_u32 s57, s55, s55
  s_add_u32 s57, s57, s48
  s_and_b32 s57, s57, 3
  s_lshl2_add_u32 s57, s54, s57
  s_lshl_b32 s57, s57, 12
  s_add_u32 s57, s57, 1888256
  s_movk_i32 s59, 0x7fff
.Lgpoll_30:
  buffer_load_dwordx4 v[8:11], v5, s[4:7], s57 offen sc1
  buffer_load_dwordx4 v[12:15], v5, s[4:7], s57 offen offset:512 sc1
  buffer_load_dwordx4 v[16:19], v5, s[4:7], s57 offen offset:1024 sc1
  buffer_load_dwordx4 v[20:23], v7, s[4:7], s57 offen offset:1536 sc1
  s_waitcnt vmcnt(3)
  v_cmpx_eq_u32 vcc, s50, v9
  v_cmpx_eq_u32 vcc, s50, v11
  s_waitcnt vmcnt(2)
  v_cmpx_eq_u32 vcc, s50, v13
  v_cmpx_eq_u32 vcc, s50, v15
  s_waitcnt vmcnt(1)
  v_cmpx_eq_u32 vcc, s50, v17
  v_cmpx_eq_u32 vcc, s50, v19
  s_waitcnt vmcnt(0)
  v_cmpx_eq_u32 vcc, s50, v21
  v_cmpx_eq_u32 vcc, s50, v23
  s_cmp_eq_u64 exec, -1
  s_mov_b64 exec, -1
  s_cbranch_scc1 .Lgdone_31
  s_sleep 1
  s_sub_u32 s59, s59, 1
  s_cbranch_scc0 .Lgpoll_30
.Lgdone_31:
  v_cmp_gt_u32 vcc, 32, v0
  v_and_b32 v9, 0xffff0000, v8
  v_lshlrev_b32 v8, 16, v8
  v_and_b32 v13, 0xffff0000, v12
  v_lshlrev_b32 v12, 16, v12
  v_and_b32 v17, 0xffff0000, v16
  v_lshlrev_b32 v16, 16, v16
  v_and_b32 v21, 0xffff0000, v20
  v_lshlrev_b32 v20, 16, v20
  v_add_f32 v11, v8, v9
  v_add_f32 v15, v12, v13
  v_add_f32 v19, v16, v17
  v_add_f32 v23, v20, v21
  v_cndmask_b32 v23, 0, v23, vcc
  v_add_f32 v11, v11, v15
  v_add_f32 v19, v19, v23
  v_add_f32 v24, v11, v19
  v_mov_b32 v25, v24
  s_nop 1
  v_permlane32_swap_b32 v24, v25
  v_add_f32 v24, v24, v25
  v_mul_f32 v24, 0x3e124925, v24
  v_sub_f32 v11, v8, v24
  v_sub_f32 v15, v12, v24
  v_sub_f32 v19, v16, v24
  v_sub_f32 v23, v20, v24
  v_fma_f32 v9, 2.0, v9, v11
  v_mul_f32 v11, 0x42800000, v11
  v_fma_f32 v13, 2.0, v13, v15
  v_mul_f32 v15, 0x42800000, v15
  v_fma_f32 v17, 2.0, v17, v19
  v_mul_f32 v19, 0x42800000, v19
  v_fma_f32 v21, 2.0, v21, v23
  v_mul_f32 v23, 0x42800000, v23
  v_fmac_f32 v10, v9, v11
  v_fmac_f32 v14, v13, v15
  v_fmac_f32 v18, v17, v19
  v_fmac_f32 v22, v21, v23
  v_cndmask_b32 v22, 0, v22, vcc
  v_add_f32 v10, v10, v14
  v_add_f32 v18, v18, v22
  v_add_f32 v26, v10, v18
  v_mov_b32 v25, v26
  s_nop 1
  v_permlane32_swap_b32 v26, v25
  v_add_f32 v26, v26, v25
  v_mul_f32 v26, 0x3b124925, v26
  v_maximum3_f32 v26, v26, 0, 0
  v_cvt_pk_bf16_f32 v26, v26, v26
  v_and_b32 v26, 0xffff0000, v26
  v_add_f32 v26, 0x3727c5ac, v26
  v_cvt_pk_bf16_f32 v26, v26, v26
  v_and_b32 v26, 0xffff0000, v26
  v_sqrt_f32 v26, v26
  s_nop 0
  v_cvt_pk_bf16_f32 v26, v26, v26
  v_and_b32 v26, 0xffff0000, v26
  v_cvt_pk_bf16_f32 v24, v24, v24
  v_and_b32 v24, 0xffff0000, v24
  v_rcp_f32 v27, v26
  v_sub_f32 v8, v8, v24
  v_sub_f32 v12, v12, v24
  v_sub_f32 v16, v16, v24
  v_sub_f32 v20, v20, v24
  v_cndmask_b32 v20, v26, v20, vcc
  v_mul_f32 v30, v26, v26
  v_mul_f32 v28, 0xc0135761, v27
  v_mul_f32 v29, v27, v27
  v_mul_f32 v29, v29, v28
  v_mul_f32 v29, 0x3d372713, v29
  v_cvt_pk_bf16_f32 v32, v8, v8
  v_cvt_pk_bf16_f32 v33, v12, v12
  v_cvt_pk_bf16_f32 v34, v16, v16
  v_cvt_pk_bf16_f32 v35, v20, v20
  s_lshl_b32 s57, s48, 10
  s_add_u32 s57, s57, 114688
  v_add_u32 v5, s57, v1
  s_lshl_b32 s57, s48, 9
  s_add_u32 s57, s57, 118784
  v_add_u32 v7, s57, v7
  ds_write_b128 v5, v[32:35]
  ds_write_b128 v7, v[28:31]
  s_branch .Lffn
.Lh1chk:
  s_mov_b32 s57, 2139193217
  v_cmpx_ne_u32 vcc, s57, v24
  v_cmpx_ne_u32 vcc, s57, v25
  v_cmpx_ne_u32 vcc, s57, v26
  v_cmpx_ne_u32 vcc, s57, v27
  v_cmpx_ne_u32 vcc, s57, v28
  v_cmpx_ne_u32 vcc, s57, v29
  v_cmpx_ne_u32 vcc, s57, v30
  v_cmpx_ne_u32 vcc, s57, v31
  v_cmpx_ne_u32 vcc, s57, v32
  v_cmpx_ne_u32 vcc, s57, v33
  v_cmpx_ne_u32 vcc, s57, v34
  v_cmpx_ne_u32 vcc, s57, v35
  v_cmpx_ne_u32 vcc, s57, v36
  v_cmpx_ne_u32 vcc, s57, v37
  v_cmpx_ne_u32 vcc, s57, v38
  v_cmpx_ne_u32 vcc, s57, v39
  v_cmpx_ne_u32 vcc, s57, v40
  v_cmpx_ne_u32 vcc, s57, v41
  v_cmpx_ne_u32 vcc, s57, v42
  v_cmpx_ne_u32 vcc, s57, v43
  v_cmpx_ne_u32 vcc, s57, v44
  v_cmpx_ne_u32 vcc, s57, v45
  v_cmpx_ne_u32 vcc, s57, v46
  v_cmpx_ne_u32 vcc, s57, v47
  v_cmpx_ne_u32 vcc, s57, v48
  v_cmpx_ne_u32 vcc, s57, v49
  v_cmpx_ne_u32 vcc, s57, v50
  v_cmpx_ne_u32 vcc, s57, v51
  v_cmpx_ne_u32 vcc, s57, v52
  v_cmpx_ne_u32 vcc, s57, v53
  v_cmpx_ne_u32 vcc, s57, v54
  v_cmpx_ne_u32 vcc, s57, v55
  s_cmp_eq_u64 exec, -1
  s_mov_b64 exec, -1
  s_cbranch_scc1 .Lh1ok
  s_sub_u32 s59, s59, 1
  s_cbranch_scc0 .Lh1again
  s_branch .Lh1ok
.LB2:
  s_cmp_lt_u32 s56, 28
  s_cbranch_scc1 .Lp2
  s_cmp_ge_u32 s2, 252
  s_cbranch_scc1 .Lexit
  s_sleep 12
  s_mov_b32 s20, s38
  s_and_b32 s21, s39, 0xffff
  s_mov_b32 s22, 1605632
  s_mov_b32 s23, 0x20000
  s_mul_i32 s57, s32, 57344
  s_mul_i32 s3, s49, 14336
  s_add_u32 s57, s57, s3
  s_add_u32 m0, s3, 73728
  s_nop 0
  buffer_load_dwordx4 v1, s[20:23], s57 offen lds
  buffer_load_dwordx4 v1, s[20:23], s57 offen offset:1024 lds
  buffer_load_dwordx4 v1, s[20:23], s57 offen offset:2048 lds
  buffer_load_dwordx4 v1, s[20:23], s57 offen offset:3072 lds
  s_add_u32 m0, m0, 4096
  s_add_u32 s57, s57, 4096
  buffer_load_dwordx4 v1, s[20:23], s57 offen lds
  buffer_load_dwordx4 v1, s[20:23], s57 offen offset:1024 lds
  buffer_load_dwordx4 v1, s[20:23], s57 offen offset:2048 lds
  buffer_load_dwordx4 v1, s[20:23], s57 offen offset:3072 lds
  s_add_u32 m0, m0, 4096
  s_add_u32 s57, s57, 4096
  buffer_load_dwordx4 v1, s[20:23], s57 offen lds
  buffer_load_dwordx4 v1, s[20:23], s57 offen offset:1024 lds
  buffer_load_dwordx4 v1, s[20:23], s57 offen offset:2048 lds
  buffer_load_dwordx4 v1, s[20:23], s57 offen offset:3072 lds
  s_add_u32 m0, m0, 4096
  s_add_u32 s57, s57, 4096
  buffer_load_dwordx4 v1, s[20:23], s57 offen lds
  buffer_load_dwordx4 v1, s[20:23], s57 offen offset:1024 lds
  v_bfe_u32 v5, v0, 2, 2
  v_lshrrev_b32 v6, 5, v0
  v_lshl_add_u32 v5, v6, 2, v5
  v_mul_u32_u24 v5, 896, v5
  v_bfe_u32 v6, v0, 4, 1
  v_lshl_add_u32 v5, v6, 5, v5
  v_and_b32 v6, 3, v0
  v_lshl_add_u32 v5, v6, 3, v5
  s_mul_i32 s57, s49, 14336
  s_add_u32 s57, s57, 73728
  v_add_u32 v5, s57, v5
  s_lshl2_add_u32 s58, s32, s49
  s_lshl_b32 s57, s58, 11
  s_add_u32 s57, s57, 12636160
  s_waitcnt vmcnt(0)
  ds_read_b64_tr_b16 v[8:9], v5
  ds_read_b64_tr_b16 v[10:11], v5 offset:7168
  ds_read_b64_tr_b16 v[12:13], v5 offset:64
  ds_read_b64_tr_b16 v[14:15], v5 offset:7232
  ds_read_b64_tr_b16 v[16:17], v5 offset:128
  ds_read_b64_tr_b16 v[18:19], v5 offset:7296
  ds_read_b64_tr_b16 v[20:21], v5 offset:192
  ds_read_b64_tr_b16 v[22:23], v5 offset:7360
  ds_read_b64_tr_b16 v[24:25], v5 offset:256
  ds_read_b64_tr_b16 v[26:27], v5 offset:7424
  ds_read_b64_tr_b16 v[28:29], v5 offset:320
  ds_read_b64_tr_b16 v[30:31], v5 offset:7488
  ds_read_b64_tr_b16 v[32:33], v5 offset:384
  ds_read_b64_tr_b16 v[34:35], v5 offset:7552
  ds_read_b64_tr_b16 v[36:37], v5 offset:448
  ds_read_b64_tr_b16 v[38:39], v5 offset:7616
  ds_read_b64_tr_b16 v[40:41], v5 offset:512
  ds_read_b64_tr_b16 v[42:43], v5 offset:7680
  ds_read_b64_tr_b16 v[44:45], v5 offset:576
  ds_read_b64_tr_b16 v[46:47], v5 offset:7744
  ds_read_b64_tr_b16 v[48:49], v5 offset:640
  ds_read_b64_tr_b16 v[50:51], v5 offset:7808
  ds_read_b64_tr_b16 v[52:53], v5 offset:704
  ds_read_b64_tr_b16 v[54:55], v5 offset:7872
  ds_read_b64_tr_b16 v[56:57], v5 offset:768
  ds_read_b64_tr_b16 v[58:59], v5 offset:7936
  ds_read_b64_tr_b16 v[60:61], v5 offset:832
  ds_read_b64_tr_b16 v[62:63], v5 offset:8000
  s_waitcnt lgkmcnt(0)
  buffer_store_dwordx4 v[8:11], v1, s[4:7], s57 offen sc1
  buffer_store_dwordx4 v[12:15], v1, s[4:7], s57 offen offset:1024 sc1
  s_add_u32 s57, s57, 229376
  buffer_store_dwordx4 v[16:19], v1, s[4:7], s57 offen sc1
  buffer_store_dwordx4 v[20:23], v1, s[4:7], s57 offen offset:1024 sc1
  s_add_u32 s57, s57, 229376
  buffer_store_dwordx4 v[24:27], v1, s[4:7], s57 offen sc1
  buffer_store_dwordx4 v[28:31], v1, s[4:7], s57 offen offset:1024 sc1
  s_add_u32 s57, s57, 229376
  buffer_store_dwordx4 v[32:35], v1, s[4:7], s57 offen sc1
  buffer_store_dwordx4 v[36:39], v1, s[4:7], s57 offen offset:1024 sc1
  s_add_u32 s57, s57, 229376
  buffer_store_dwordx4 v[40:43], v1, s[4:7], s57 offen sc1
  buffer_store_dwordx4 v[44:47], v1, s[4:7], s57 offen offset:1024 sc1
  s_add_u32 s57, s57, 229376
  buffer_store_dwordx4 v[48:51], v1, s[4:7], s57 offen sc1
  buffer_store_dwordx4 v[52:55], v1, s[4:7], s57 offen offset:1024 sc1
  s_add_u32 s57, s57, 229376
  buffer_store_dwordx4 v[56:59], v1, s[4:7], s57 offen sc1
  buffer_store_dwordx4 v[60:63], v1, s[4:7], s57 offen offset:1024 sc1
  v_cmp_eq_u32 vcc, 0, v0
  v_mov_b32 v5, 0
  s_lshl_b32 s58, s58, 2
  v_cndmask_b32 v5, v4, v5, vcc
  s_add_u32 s58, s58, 9216
  s_waitcnt vmcnt(0)
  buffer_store_dword v2, v5, s[4:7], s58 offen sc1
  s_branch .Lexit
.Lp2:
  s_cmp_lt_u32 s2, 98
  s_cbranch_scc1 .Lu
  s_sleep 8
  s_waitcnt vmcnt(0)
  s_branch .Lnou
.Lu:
  v_lshrrev_b32 v6, 5, v0
  v_lshlrev_b32 v5, 4, v6
  v_add_u32 v5, 163584, v5
  ds_read_b128 v[24:27], v5 offset:128
  ds_read_b128 v[40:43], v5
  ds_read_b128 v[28:31], v5 offset:160
  ds_read_b128 v[44:47], v5 offset:32
  ds_read_b128 v[32:35], v5 offset:192
  ds_read_b128 v[48:51], v5 offset:64
  ds_read_b128 v[36:39], v5 offset:224
  ds_read_b128 v[52:55], v5 offset:96
  v_mov_b32 v8, 0
  v_mov_b32 v9, 0
  v_mov_b32 v10, 0
  v_mov_b32 v11, 0
  s_lshl2_add_u32 s57, s33, s49
  s_lshl4_add_u32 s57, s57, s32
  s_lshl_b32 s57, s57, 9
  s_add_u32 s57, s57, 2150400
  v_lshlrev_b32 v5, 2, v3
  v_lshl_or_b32 v5, v6, 12, v5
  s_waitcnt lgkmcnt(0)
  v_dot2c_f32_bf16 v8, v64, v24
  v_dot2c_f32_bf16 v10, v64, v40
  v_dot2c_f32_bf16 v9, v80, v24
  v_dot2c_f32_bf16 v11, v80, v40
  v_dot2c_f32_bf16 v8, v65, v25
  v_dot2c_f32_bf16 v10, v65, v41
  v_dot2c_f32_bf16 v9, v81, v25
  v_dot2c_f32_bf16 v11, v81, v41
  v_dot2c_f32_bf16 v8, v66, v26
  v_dot2c_f32_bf16 v10, v66, v42
  v_dot2c_f32_bf16 v9, v82, v26
  v_dot2c_f32_bf16 v11, v82, v42
  v_dot2c_f32_bf16 v8, v67, v27
  v_dot2c_f32_bf16 v10, v67, v43
  v_dot2c_f32_bf16 v9, v83, v27
  v_dot2c_f32_bf16 v11, v83, v43
  v_dot2c_f32_bf16 v8, v68, v28
  v_dot2c_f32_bf16 v10, v68, v44
  v_dot2c_f32_bf16 v9, v84, v28
  v_dot2c_f32_bf16 v11, v84, v44
  v_dot2c_f32_bf16 v8, v69, v29
  v_dot2c_f32_bf16 v10, v69, v45
  v_dot2c_f32_bf16 v9, v85, v29
  v_dot2c_f32_bf16 v11, v85, v45
  v_dot2c_f32_bf16 v8, v70, v30
  v_dot2c_f32_bf16 v10, v70, v46
  v_dot2c_f32_bf16 v9, v86, v30
  v_dot2c_f32_bf16 v11, v86, v46
  v_dot2c_f32_bf16 v8, v71, v31
  v_dot2c_f32_bf16 v10, v71, v47
  v_dot2c_f32_bf16 v9, v87, v31
  v_dot2c_f32_bf16 v11, v87, v47
  v_dot2c_f32_bf16 v8, v72, v32
  v_dot2c_f32_bf16 v10, v72, v48
  v_dot2c_f32_bf16 v9, v88, v32
  v_dot2c_f32_bf16 v11, v88, v48
  v_dot2c_f32_bf16 v8, v73, v33
  v_dot2c_f32_bf16 v10, v73, v49
  v_dot2c_f32_bf16 v9, v89, v33
  v_dot2c_f32_bf16 v11, v89, v49
  v_dot2c_f32_bf16 v8, v74, v34
  v_dot2c_f32_bf16 v10, v74, v50
  v_dot2c_f32_bf16 v9, v90, v34
  v_dot2c_f32_bf16 v11, v90, v50
  v_dot2c_f32_bf16 v8, v75, v35
  v_dot2c_f32_bf16 v10, v75, v51
  v_dot2c_f32_bf16 v9, v91, v35
  v_dot2c_f32_bf16 v11, v91, v51
  v_dot2c_f32_bf16 v8, v76, v36
  v_dot2c_f32_bf16 v10, v76, v52
  v_dot2c_f32_bf16 v9, v92, v36
  v_dot2c_f32_bf16 v11, v92, v52
  v_dot2c_f32_bf16 v8, v77, v37
  v_dot2c_f32_bf16 v10, v77, v53
  v_dot2c_f32_bf16 v9, v93, v37
  v_dot2c_f32_bf16 v11, v93, v53
  v_dot2c_f32_bf16 v8, v78, v38
  v_dot2c_f32_bf16 v10, v78, v54
  v_dot2c_f32_bf16 v9, v94, v38
  v_dot2c_f32_bf16 v11, v94, v54
  v_dot2c_f32_bf16 v8, v79, v39
  v_dot2c_f32_bf16 v10, v79, v55
  v_dot2c_f32_bf16 v9, v95, v39
  v_dot2c_f32_bf16 v11, v95, v55
  s_nop 1
  v_permlane32_swap_b32 v8, v9
  v_permlane32_swap_b32 v10, v11
  v_add_f32 v8, v8, v9
  v_add_f32 v10, v10, v11
  v_mov_b32 v9, v2
  v_mov_b32 v11, v2
  buffer_store_dwordx4 v[8:11], v5, s[4:7], s57 offen sc1
.Lnou:
  s_waitcnt vmcnt(1)
  v_cmp_ne_u32 vcc, s50, v57
  s_cbranch_vccz .Ldone_33
  s_movk_i32 s59, 0x7fff
.Lpoll_32:
  buffer_load_dword v57, v56, s[4:7], s22 offen sc1
  s_waitcnt vmcnt(0)
  v_cmp_ne_u32 vcc, s50, v57
  s_cbranch_vccz .Ldone_33
  s_sleep 1
  s_sub_u32 s59, s59, 1
  s_cbranch_scc0 .Lpoll_32
.Ldone_33:
  s_add_u32 s57, s21, 0
  buffer_load_dwordx4 v[128:131], v1, s[4:7], s57 offen sc1
  buffer_load_dwordx4 v[132:135], v1, s[4:7], s57 offen offset:1024 sc1
  buffer_load_dwordx4 v[136:139], v1, s[4:7], s57 offen offset:2048 sc1
  buffer_load_dwordx4 v[140:143], v1, s[4:7], s57 offen offset:3072 sc1
  s_add_u32 s57, s21, 4096
  buffer_load_dwordx4 v[144:147], v1, s[4:7], s57 offen sc1
  buffer_load_dwordx4 v[148:151], v1, s[4:7], s57 offen offset:1024 sc1
  buffer_load_dwordx4 v[152:155], v1, s[4:7], s57 offen offset:2048 sc1
  buffer_load_dwordx4 v[156:159], v1, s[4:7], s57 offen offset:3072 sc1
  s_add_u32 s57, s21, 8192
  buffer_load_dwordx4 v[160:163], v1, s[4:7], s57 offen sc1
  buffer_load_dwordx4 v[164:167], v1, s[4:7], s57 offen offset:1024 sc1
  buffer_load_dwordx4 v[168:171], v1, s[4:7], s57 offen offset:2048 sc1
  buffer_load_dwordx4 v[172:175], v1, s[4:7], s57 offen offset:3072 sc1
  s_mov_b32 s3, s43
  s_lshl_b32 s0, s3, 14
  s_add_u32 s0, s0, 5492736
  s_lshr_b32 s57, s3, 1
  s_and_b32 s3, s3, 1
  s_lshl_b32 s3, s3, 8
  s_mul_i32 s32, s57, 7
  s_add_u32 s32, s32, s53
  s_lshl_b32 s32, s32, 12
  s_add_u32 s32, s32, s3
  s_add_u32 s32, s32, 7589888
  s_lshl3_add_u32 s33, s57, s53
  s_lshl_b32 s33, s33, 9
  s_add_u32 s33, s33, s3
  s_add_u32 s33, s33, 1888256
  s_and_b32 s57, s53, 3
  s_xor_b32 s12, s57, 0
  s_lshl_b32 s12, s12, 12
  s_add_u32 s12, s12, s0
  s_xor_b32 s13, s57, 1
  s_lshl_b32 s13, s13, 12
  s_add_u32 s13, s13, s0
  s_xor_b32 s14, s57, 2
  s_lshl_b32 s14, s14, 12
  s_add_u32 s14, s14, s0
  s_xor_b32 s15, s57, 3
  s_lshl_b32 s15, s15, 12
  s_add_u32 s15, s15, s0
  s_setprio 1
  s_cmp_lg_u32 s49, 3
  s_cbranch_scc1 .Lp2go
  v_and_b32 v5, 15, v0
  v_lshlrev_b32 v5, 7, v5
  s_lshl_b32 s57, s54, 11
  s_add_u32 s57, s57, 176128
  s_movk_i32 s59, 0x7fff
.Lpoll_35:
  buffer_load_dword v6, v5, s[4:7], s57 offen sc1
  s_waitcnt vmcnt(0)
  v_cmp_ne_u32 vcc, s50, v6
  s_cbranch_vccz .Ldone_36
  s_sleep 1
  s_sub_u32 s59, s59, 1
  s_cbranch_scc0 .Lpoll_35
.Ldone_36:
.Lp2go:
  s_movk_i32 s59, 0x7fff
  s_barrier
.Lp2again:
  buffer_load_dwordx4 v[48:51], v1, s[4:7], s12 offen sc1
  buffer_load_dwordx4 v[52:55], v1, s[4:7], s12 offen offset:1024 sc1
  buffer_load_dwordx4 v[56:59], v1, s[4:7], s12 offen offset:2048 sc1
  buffer_load_dwordx4 v[60:63], v1, s[4:7], s12 offen offset:3072 sc1
  buffer_load_dwordx4 v[64:67], v1, s[4:7], s13 offen sc1
  buffer_load_dwordx4 v[68:71], v1, s[4:7], s13 offen offset:1024 sc1
  buffer_load_dwordx4 v[72:75], v1, s[4:7], s13 offen offset:2048 sc1
  buffer_load_dwordx4 v[76:79], v1, s[4:7], s13 offen offset:3072 sc1
  buffer_load_dwordx4 v[80:83], v1, s[4:7], s14 offen sc1
  buffer_load_dwordx4 v[84:87], v1, s[4:7], s14 offen offset:1024 sc1
  buffer_load_dwordx4 v[88:91], v1, s[4:7], s14 offen offset:2048 sc1
  buffer_load_dwordx4 v[92:95], v1, s[4:7], s14 offen offset:3072 sc1
  buffer_load_dwordx4 v[96:99], v1, s[4:7], s15 offen sc1
  buffer_load_dwordx4 v[100:103], v1, s[4:7], s15 offen offset:1024 sc1
  buffer_load_dwordx4 v[104:107], v1, s[4:7], s15 offen offset:2048 sc1
  buffer_load_dwordx4 v[108:111], v1, s[4:7], s15 offen offset:3072 sc1
  ds_read_b128 v[24:27], v245
  ds_read_b128 v[28:31], v245 offset:1024
  ds_read_b128 v[32:35], v245 offset:2048
  ds_read_b128 v[36:39], v245 offset:3072
  s_waitcnt vmcnt(15) lgkmcnt(2)
  v_mfma_f32_16x16x32_bf16 v[8:11], v[24:27], v[48:51], 0
  ds_read_b128 v[40:43], v245 offset:4096
  v_mfma_f32_16x16x32_bf16 v[12:15], v[28:31], v[48:51], 0
  ds_read_b128 v[44:47], v245 offset:5120
  s_waitcnt lgkmcnt(2)
  v_mfma_f32_16x16x32_bf16 v[16:19], v[32:35], v[48:51], 0
  ds_read_b128 v[24:27], v245 offset:6144
  v_mfma_f32_16x16x32_bf16 v[20:23], v[36:39], v[48:51], 0
  ds_read_b128 v[28:31], v245 offset:7168
  s_waitcnt vmcnt(14) lgkmcnt(2)
  v_mfma_f32_16x16x32_bf16 v[8:11], v[40:43], v[52:55], v[8:11]
  ds_read_b128 v[32:35], v245 offset:8192
  v_mfma_f32_16x16x32_bf16 v[12:15], v[44:47], v[52:55], v[12:15]
  ds_read_b128 v[36:39], v245 offset:9216
  s_waitcnt lgkmcnt(2)
  v_mfma_f32_16x16x32_bf16 v[16:19], v[24:27], v[52:55], v[16:19]
  ds_read_b128 v[40:43], v245 offset:10240
  v_mfma_f32_16x16x32_bf16 v[20:23], v[28:31], v[52:55], v[20:23]
  ds_read_b128 v[44:47], v245 offset:11264
  s_waitcnt vmcnt(13) lgkmcnt(2)
  v_mfma_f32_16x16x32_bf16 v[8:11], v[32:35], v[56:59], v[8:11]
  ds_read_b128 v[24:27], v245 offset:12288
  v_mfma_f32_16x16x32_bf16 v[12:15], v[36:39], v[56:59], v[12:15]
  ds_read_b128 v[28:31], v245 offset:13312
  s_waitcnt lgkmcnt(2)
  v_mfma_f32_16x16x32_bf16 v[16:19], v[40:43], v[56:59], v[16:19]
  ds_read_b128 v[32:35], v245 offset:14336
  v_mfma_f32_16x16x32_bf16 v[20:23], v[44:47], v[56:59], v[20:23]
  ds_read_b128 v[36:39], v245 offset:15360
  s_waitcnt vmcnt(12) lgkmcnt(2)
  v_mfma_f32_16x16x32_bf16 v[8:11], v[24:27], v[60:63], v[8:11]
  ds_read_b128 v[40:43], v245 offset:16384
  v_mfma_f32_16x16x32_bf16 v[12:15], v[28:31], v[60:63], v[12:15]
  ds_read_b128 v[44:47], v245 offset:17408
  s_waitcnt lgkmcnt(2)
  v_mfma_f32_16x16x32_bf16 v[16:19], v[32:35], v[60:63], v[16:19]
  ds_read_b128 v[24:27], v245 offset:18432
  v_mfma_f32_16x16x32_bf16 v[20:23], v[36:39], v[60:63], v[20:23]
  ds_read_b128 v[28:31], v245 offset:19456
  s_waitcnt vmcnt(11) lgkmcnt(2)
  v_mfma_f32_16x16x32_bf16 v[8:11], v[40:43], v[64:67], v[8:11]
  ds_read_b128 v[32:35], v245 offset:20480
  v_mfma_f32_16x16x32_bf16 v[12:15], v[44:47], v[64:67], v[12:15]
  ds_read_b128 v[36:39], v245 offset:21504
  s_waitcnt lgkmcnt(2)
  v_mfma_f32_16x16x32_bf16 v[16:19], v[24:27], v[64:67], v[16:19]
  ds_read_b128 v[40:43], v245 offset:22528
  v_mfma_f32_16x16x32_bf16 v[20:23], v[28:31], v[64:67], v[20:23]
  ds_read_b128 v[44:47], v245 offset:23552
  s_waitcnt vmcnt(10) lgkmcnt(2)
  v_mfma_f32_16x16x32_bf16 v[8:11], v[32:35], v[68:71], v[8:11]
  ds_read_b128 v[24:27], v245 offset:24576
  v_mfma_f32_16x16x32_bf16 v[12:15], v[36:39], v[68:71], v[12:15]
  ds_read_b128 v[28:31], v245 offset:25600
  s_waitcnt lgkmcnt(2)
  v_mfma_f32_16x16x32_bf16 v[16:19], v[40:43], v[68:71], v[16:19]
  ds_read_b128 v[32:35], v245 offset:26624
  v_mfma_f32_16x16x32_bf16 v[20:23], v[44:47], v[68:71], v[20:23]
  ds_read_b128 v[36:39], v245 offset:27648
  s_waitcnt vmcnt(9) lgkmcnt(2)
  v_mfma_f32_16x16x32_bf16 v[8:11], v[24:27], v[72:75], v[8:11]
  ds_read_b128 v[40:43], v245 offset:28672
  v_mfma_f32_16x16x32_bf16 v[12:15], v[28:31], v[72:75], v[12:15]
  ds_read_b128 v[44:47], v245 offset:29696
  s_waitcnt lgkmcnt(2)
  v_mfma_f32_16x16x32_bf16 v[16:19], v[32:35], v[72:75], v[16:19]
  ds_read_b128 v[24:27], v245 offset:30720
  v_mfma_f32_16x16x32_bf16 v[20:23], v[36:39], v[72:75], v[20:23]
  ds_read_b128 v[28:31], v245 offset:31744
  s_waitcnt vmcnt(8) lgkmcnt(2)
  v_mfma_f32_16x16x32_bf16 v[8:11], v[40:43], v[76:79], v[8:11]
  ds_read_b128 v[32:35], v245 offset:32768
  v_mfma_f32_16x16x32_bf16 v[12:15], v[44:47], v[76:79], v[12:15]
  ds_read_b128 v[36:39], v245 offset:33792
  s_waitcnt lgkmcnt(2)
  v_mfma_f32_16x16x32_bf16 v[16:19], v[24:27], v[76:79], v[16:19]
  ds_read_b128 v[40:43], v245 offset:34816
  v_mfma_f32_16x16x32_bf16 v[20:23], v[28:31], v[76:79], v[20:23]
  ds_read_b128 v[44:47], v245 offset:35840
  s_waitcnt vmcnt(7) lgkmcnt(2)
  v_mfma_f32_16x16x32_bf16 v[8:11], v[32:35], v[80:83], v[8:11]
  ds_read_b128 v[24:27], v245 offset:36864
  v_mfma_f32_16x16x32_bf16 v[12:15], v[36:39], v[80:83], v[12:15]
  ds_read_b128 v[28:31], v245 offset:37888
  s_waitcnt lgkmcnt(2)
  v_mfma_f32_16x16x32_bf16 v[16:19], v[40:43], v[80:83], v[16:19]
  ds_read_b128 v[32:35], v245 offset:38912
  v_mfma_f32_16x16x32_bf16 v[20:23], v[44:47], v[80:83], v[20:23]
  ds_read_b128 v[36:39], v245 offset:39936
  s_waitcnt vmcnt(6) lgkmcnt(2)
  v_mfma_f32_16x16x32_bf16 v[8:11], v[24:27], v[84:87], v[8:11]
  ds_read_b128 v[40:43], v245 offset:40960
  v_mfma_f32_16x16x32_bf16 v[12:15], v[28:31], v[84:87], v[12:15]
  ds_read_b128 v[44:47], v245 offset:41984
  s_waitcnt lgkmcnt(2)
  v_mfma_f32_16x16x32_bf16 v[16:19], v[32:35], v[84:87], v[16:19]
  ds_read_b128 v[24:27], v245 offset:43008
  v_mfma_f32_16x16x32_bf16 v[20:23], v[36:39], v[84:87], v[20:23]
  ds_read_b128 v[28:31], v245 offset:44032
  s_waitcnt vmcnt(5) lgkmcnt(2)
  v_mfma_f32_16x16x32_bf16 v[8:11], v[40:43], v[88:91], v[8:11]
  ds_read_b128 v[32:35], v245 offset:45056
  v_mfma_f32_16x16x32_bf16 v[12:15], v[44:47], v[88:91], v[12:15]
  ds_read_b128 v[36:39], v245 offset:46080
  s_waitcnt lgkmcnt(2)
  v_mfma_f32_16x16x32_bf16 v[16:19], v[24:27], v[88:91], v[16:19]
  ds_read_b128 v[40:43], v245 offset:47104
  v_mfma_f32_16x16x32_bf16 v[20:23], v[28:31], v[88:91], v[20:23]
  ds_read_b128 v[44:47], v245 offset:48128
  s_waitcnt vmcnt(4) lgkmcnt(2)
  v_mfma_f32_16x16x32_bf16 v[8:11], v[32:35], v[92:95], v[8:11]
  ds_read_b128 v[24:27], v245 offset:49152
  v_mfma_f32_16x16x32_bf16 v[12:15], v[36:39], v[92:95], v[12:15]
  ds_read_b128 v[28:31], v245 offset:50176
  s_waitcnt lgkmcnt(2)
  v_mfma_f32_16x16x32_bf16 v[16:19], v[40:43], v[92:95], v[16:19]
  ds_read_b128 v[32:35], v245 offset:51200
  v_mfma_f32_16x16x32_bf16 v[20:23], v[44:47], v[92:95], v[20:23]
  ds_read_b128 v[36:39], v245 offset:52224
  s_waitcnt vmcnt(3) lgkmcnt(2)
  v_mfma_f32_16x16x32_bf16 v[8:11], v[24:27], v[96:99], v[8:11]
  ds_read_b128 v[40:43], v245 offset:53248
  v_mfma_f32_16x16x32_bf16 v[12:15], v[28:31], v[96:99], v[12:15]
  ds_read_b128 v[44:47], v245 offset:54272
  s_waitcnt lgkmcnt(2)
  v_mfma_f32_16x16x32_bf16 v[16:19], v[32:35], v[96:99], v[16:19]
  ds_read_b128 v[24:27], v245 offset:55296
  v_mfma_f32_16x16x32_bf16 v[20:23], v[36:39], v[96:99], v[20:23]
  ds_read_b128 v[28:31], v245 offset:56320
  s_waitcnt vmcnt(2) lgkmcnt(2)
  v_mfma_f32_16x16x32_bf16 v[8:11], v[40:43], v[100:103], v[8:11]
  ds_read_b128 v[32:35], v245 offset:57344
  v_mfma_f32_16x16x32_bf16 v[12:15], v[44:47], v[100:103], v[12:15]
  ds_read_b128 v[36:39], v245 offset:58368
  s_waitcnt lgkmcnt(2)
  v_mfma_f32_16x16x32_bf16 v[16:19], v[24:27], v[100:103], v[16:19]
  ds_read_b128 v[40:43], v245 offset:59392
  v_mfma_f32_16x16x32_bf16 v[20:23], v[28:31], v[100:103], v[20:23]
  ds_read_b128 v[44:47], v245 offset:60416
  s_waitcnt vmcnt(1) lgkmcnt(2)
  v_mfma_f32_16x16x32_bf16 v[8:11], v[32:35], v[104:107], v[8:11]
  ds_read_b128 v[24:27], v245 offset:61440
  v_mfma_f32_16x16x32_bf16 v[12:15], v[36:39], v[104:107], v[12:15]
  ds_read_b128 v[28:31], v245 offset:62464
  s_waitcnt lgkmcnt(2)
  v_mfma_f32_16x16x32_bf16 v[16:19], v[40:43], v[104:107], v[16:19]
  ds_read_b128 v[32:35], v245 offset:63488
  v_mfma_f32_16x16x32_bf16 v[20:23], v[44:47], v[104:107], v[20:23]
  ds_read_b128 v[36:39], v245 offset:64512
  s_waitcnt vmcnt(0) lgkmcnt(2)
  v_mfma_f32_16x16x32_bf16 v[8:11], v[24:27], v[108:111], v[8:11]
  ds_read_b128 v[40:43], v247
  v_mfma_f32_16x16x32_bf16 v[12:15], v[28:31], v[108:111], v[12:15]
  ds_read_b128 v[44:47], v247 offset:16
  s_waitcnt lgkmcnt(2)
  v_mfma_f32_16x16x32_bf16 v[16:19], v[32:35], v[108:111], v[16:19]
  ds_read_b128 v[24:27], v247 offset:32
  v_mfma_f32_16x16x32_bf16 v[20:23], v[36:39], v[108:111], v[20:23]
  ds_read_b128 v[28:31], v247 offset:48
  v_cmp_u_f32 vcc, v8, v8
  s_cbranch_vccnz .Lp2chk
.Lp2ok:
  ds_read_b128 v[48:51], v247 offset:256
  ds_read_b128 v[52:55], v247 offset:272
  ds_read_b128 v[56:59], v247 offset:288
  ds_read_b128 v[60:63], v247 offset:304
  s_waitcnt lgkmcnt(4)
  v_pk_fma_f32 v[112:113], v[40:41], v[8:9], v[112:113]
  v_pk_fma_f32 v[114:115], v[42:43], v[10:11], v[114:115]
  v_pk_fma_f32 v[116:117], v[44:45], v[12:13], v[116:117]
  v_pk_fma_f32 v[118:119], v[46:47], v[14:15], v[118:119]
  v_pk_fma_f32 v[120:121], v[24:25], v[16:17], v[120:121]
  v_pk_fma_f32 v[122:123], v[26:27], v[18:19], v[122:123]
  v_pk_fma_f32 v[124:125], v[28:29], v[20:21], v[124:125]
  v_pk_fma_f32 v[126:127], v[30:31], v[22:23], v[126:127]
  s_waitcnt lgkmcnt(0)
  v_pk_mul_f32 v[8:9], v[112:113], v[48:49]
  v_pk_mul_f32 v[10:11], v[114:115], v[50:51]
  v_pk_mul_f32 v[12:13], v[116:117], v[52:53]
  v_pk_mul_f32 v[14:15], v[118:119], v[54:55]
  v_pk_mul_f32 v[16:17], v[120:121], v[56:57]
  v_pk_mul_f32 v[18:19], v[122:123], v[58:59]
  v_pk_mul_f32 v[20:21], v[124:125], v[60:61]
  v_pk_mul_f32 v[22:23], v[126:127], v[62:63]
  v_cvt_pk_bf16_f32 v64, v8, v9
  v_cvt_pk_bf16_f32 v65, v10, v11
  v_cvt_pk_bf16_f32 v66, v12, v13
  v_cvt_pk_bf16_f32 v67, v14, v15
  buffer_store_dwordx4 v[64:67], v248, s[4:7], s32 offen sc1
  v_cvt_pk_bf16_f32 v68, v16, v17
  v_cvt_pk_bf16_f32 v69, v18, v19
  v_cvt_pk_bf16_f32 v70, v20, v21
  v_cvt_pk_bf16_f32 v71, v22, v23
  buffer_store_dwordx4 v[68:71], v248, s[4:7], s32 offen offset:512 sc1
  s_barrier
  v_pk_add_f32 v[8:9], v[112:113], v[114:115]
  v_pk_mul_f32 v[10:11], v[112:113], v[112:113]
  v_pk_fma_f32 v[10:11], v[114:115], v[114:115], v[10:11]
  v_pk_add_f32 v[8:9], v[8:9], v[116:117]
  v_pk_fma_f32 v[10:11], v[116:117], v[116:117], v[10:11]
  v_pk_add_f32 v[8:9], v[8:9], v[118:119]
  v_pk_fma_f32 v[10:11], v[118:119], v[118:119], v[10:11]
  v_pk_add_f32 v[8:9], v[8:9], v[120:121]
  v_pk_fma_f32 v[10:11], v[120:121], v[120:121], v[10:11]
  v_pk_add_f32 v[8:9], v[8:9], v[122:123]
  v_pk_fma_f32 v[10:11], v[122:123], v[122:123], v[10:11]
  v_pk_add_f32 v[8:9], v[8:9], v[124:125]
  v_pk_fma_f32 v[10:11], v[124:125], v[124:125], v[10:11]
  v_pk_add_f32 v[8:9], v[8:9], v[126:127]
  v_pk_fma_f32 v[10:11], v[126:127], v[126:127], v[10:11]
  v_add_f32 v8, v8, v9
  v_add_f32 v10, v10, v11
  v_mov_b32 v9, v8
  s_nop 1
  v_permlane16_swap_b32 v8, v9
  v_add_f32 v8, v8, v9
  v_mov_b32 v11, v10
  s_nop 1
  v_permlane16_swap_b32 v10, v11
  v_add_f32 v10, v10, v11
  v_mov_b32 v9, v8
  s_nop 1
  v_permlane32_swap_b32 v8, v9
  v_add_f32 v8, v8, v9
  v_mov_b32 v11, v10
  s_nop 1
  v_permlane32_swap_b32 v10, v11
  v_add_f32 v10, v10, v11
  v_mul_f32 v8, 0x3c800000, v8
  v_mov_b32 v9, v2
  v_mov_b32 v11, v2
  v_cvt_pk_bf16_f32 v8, v244, v8
  buffer_store_dwordx4 v[8:11], v249, s[4:7], s33 offen sc1
  v_add_f32 v112, v112, v244
  v_add_f32 v113, v113, v244
  v_add_f32 v114, v114, v244
  v_add_f32 v115, v115, v244
  v_add_f32 v116, v116, v244
  v_add_f32 v117, v117, v244
  v_add_f32 v118, v118, v244
  v_add_f32 v119, v119, v244
  v_add_f32 v120, v120, v244
  v_add_f32 v121, v121, v244
  v_add_f32 v122, v122, v244
  v_add_f32 v123, v123, v244
  v_add_f32 v124, v124, v244
  v_add_f32 v125, v125, v244
  v_add_f32 v126, v126, v244
  v_add_f32 v127, v127, v244
  v_cvt_pk_bf16_f32 v72, v112, v113
  v_cvt_pk_bf16_f32 v73, v114, v115
  v_cvt_pk_bf16_f32 v74, v116, v117
  v_cvt_pk_bf16_f32 v75, v118, v119
  v_cvt_pk_bf16_f32 v76, v120, v121
  v_cvt_pk_bf16_f32 v77, v122, v123
  v_cvt_pk_bf16_f32 v78, v124, v125
  v_cvt_pk_bf16_f32 v79, v126, v127
  ds_write_b128 v246, v[72:75]
  ds_write_b128 v246, v[76:79] offset:16
  s_mul_i32 s57, s55, 7
  s_add_u32 s57, s57, s53
  s_lshl_b32 s57, s57, 7
  s_lshl_b32 s0, s54, 11
  s_add_u32 s0, s0, 208896
  s_add_u32 s57, s57, s0
  s_waitcnt vmcnt(1)
  s_barrier
  s_cmp_lg_u32 s49, 3
  s_cbranch_scc1 .Lnoyf
  buffer_store_dword v2, v3, s[4:7], s57 offen sc1
  s_cmp_lg_u32 s2, 0
  s_cbranch_scc1 .Lnoyf
  buffer_store_dword v2, v3, s[4:7], 0 offen sc1
.Lnoyf:
  s_setprio 0
  s_add_u32 s57, s21, 12288
  buffer_load_dwordx4 v[176:179], v1, s[4:7], s57 offen sc1
  buffer_load_dwordx4 v[180:183], v1, s[4:7], s57 offen offset:1024 sc1
  buffer_load_dwordx4 v[184:187], v1, s[4:7], s57 offen offset:2048 sc1
  buffer_load_dwordx4 v[188:191], v1, s[4:7], s57 offen offset:3072 sc1
  s_add_u32 s57, s21, 16384
  buffer_load_dwordx4 v[192:195], v1, s[4:7], s57 offen sc1
  buffer_load_dwordx4 v[196:199], v1, s[4:7], s57 offen offset:1024 sc1
  s_lshl_b32 s57, s20, 12
  s_add_u32 s57, s57, 2150400
  v_lshrrev_b32 v6, 5, v0
  v_lshlrev_b32 v7, 2, v3
  v_lshl_or_b32 v5, v6, 11, v7
  s_movk_i32 s59, 0x7fff
.Lgpoll_38:
  buffer_load_dwordx4 v[8:11], v5, s[4:7], s57 offen sc1
  buffer_load_dwordx4 v[12:15], v5, s[4:7], s57 offen offset:512 sc1
  buffer_load_dwordx4 v[16:19], v5, s[4:7], s57 offen offset:1024 sc1
  buffer_load_dwordx4 v[20:23], v7, s[4:7], s57 offen offset:1536 sc1
  s_waitcnt vmcnt(3)
  v_cmpx_eq_u32 vcc, s50, v9
  v_cmpx_eq_u32 vcc, s50, v11
  s_waitcnt vmcnt(2)
  v_cmpx_eq_u32 vcc, s50, v13
  v_cmpx_eq_u32 vcc, s50, v15
  s_waitcnt vmcnt(1)
  v_cmpx_eq_u32 vcc, s50, v17
  v_cmpx_eq_u32 vcc, s50, v19
  s_waitcnt vmcnt(0)
  v_cmpx_eq_u32 vcc, s50, v21
  v_cmpx_eq_u32 vcc, s50, v23
  s_cmp_eq_u64 exec, -1
  s_mov_b64 exec, -1
  s_cbranch_scc1 .Lgdone_39
  s_sleep 1
  s_sub_u32 s59, s59, 1
  s_cbranch_scc0 .Lgpoll_38
.Lgdone_39:
  v_cmp_gt_u32 vcc, 32, v0
  v_add_f32 v10, v10, v14
  v_add_f32 v10, v10, v18
  v_cndmask_b32 v22, 0, v22, vcc
  v_add_f32 v10, v10, v22
  v_mov_b32 v11, v10
  s_nop 1
  v_permlane32_swap_b32 v10, v11
  v_add_f32 v10, v10, v11
  v_cndmask_b32 v20, v10, v20, vcc
  v_cvt_pk_bf16_f32 v9, v8, v8
  v_and_b32 v9, 0xffff0000, v9
  v_sub_f32 v240, v8, v9
  v_cvt_pk_bf16_f32 v240, v9, v240
  v_cvt_pk_bf16_f32 v13, v12, v12
  v_and_b32 v13, 0xffff0000, v13
  v_sub_f32 v241, v12, v13
  v_cvt_pk_bf16_f32 v241, v13, v241
  v_cvt_pk_bf16_f32 v17, v16, v16
  v_and_b32 v17, 0xffff0000, v17
  v_sub_f32 v242, v16, v17
  v_cvt_pk_bf16_f32 v242, v17, v242
  v_cvt_pk_bf16_f32 v21, v20, v20
  v_and_b32 v21, 0xffff0000, v21
  v_sub_f32 v243, v20, v21
  v_cvt_pk_bf16_f32 v243, v21, v243
  s_branch .Lffn
.Lp2chk:
  s_mov_b32 s57, 2139193217
  v_cmpx_ne_u32 vcc, s57, v48
  v_cmpx_ne_u32 vcc, s57, v49
  v_cmpx_ne_u32 vcc, s57, v50
  v_cmpx_ne_u32 vcc, s57, v51
  v_cmpx_ne_u32 vcc, s57, v52
  v_cmpx_ne_u32 vcc, s57, v53
  v_cmpx_ne_u32 vcc, s57, v54
  v_cmpx_ne_u32 vcc, s57, v55
  v_cmpx_ne_u32 vcc, s57, v56
  v_cmpx_ne_u32 vcc, s57, v57
  v_cmpx_ne_u32 vcc, s57, v58
  v_cmpx_ne_u32 vcc, s57, v59
  v_cmpx_ne_u32 vcc, s57, v60
  v_cmpx_ne_u32 vcc, s57, v61
  v_cmpx_ne_u32 vcc, s57, v62
  v_cmpx_ne_u32 vcc, s57, v63
  v_cmpx_ne_u32 vcc, s57, v64
  v_cmpx_ne_u32 vcc, s57, v65
  v_cmpx_ne_u32 vcc, s57, v66
  v_cmpx_ne_u32 vcc, s57, v67
  v_cmpx_ne_u32 vcc, s57, v68
  v_cmpx_ne_u32 vcc, s57, v69
  v_cmpx_ne_u32 vcc, s57, v70
  v_cmpx_ne_u32 vcc, s57, v71
  v_cmpx_ne_u32 vcc, s57, v72
  v_cmpx_ne_u32 vcc, s57, v73
  v_cmpx_ne_u32 vcc, s57, v74
  v_cmpx_ne_u32 vcc, s57, v75
  v_cmpx_ne_u32 vcc, s57, v76
  v_cmpx_ne_u32 vcc, s57, v77
  v_cmpx_ne_u32 vcc, s57, v78
  v_cmpx_ne_u32 vcc, s57, v79
  v_cmpx_ne_u32 vcc, s57, v80
  v_cmpx_ne_u32 vcc, s57, v81
  v_cmpx_ne_u32 vcc, s57, v82
  v_cmpx_ne_u32 vcc, s57, v83
  v_cmpx_ne_u32 vcc, s57, v84
  v_cmpx_ne_u32 vcc, s57, v85
  v_cmpx_ne_u32 vcc, s57, v86
  v_cmpx_ne_u32 vcc, s57, v87
  v_cmpx_ne_u32 vcc, s57, v88
  v_cmpx_ne_u32 vcc, s57, v89
  v_cmpx_ne_u32 vcc, s57, v90
  v_cmpx_ne_u32 vcc, s57, v91
  v_cmpx_ne_u32 vcc, s57, v92
  v_cmpx_ne_u32 vcc, s57, v93
  v_cmpx_ne_u32 vcc, s57, v94
  v_cmpx_ne_u32 vcc, s57, v95
  v_cmpx_ne_u32 vcc, s57, v96
  v_cmpx_ne_u32 vcc, s57, v97
  v_cmpx_ne_u32 vcc, s57, v98
  v_cmpx_ne_u32 vcc, s57, v99
  v_cmpx_ne_u32 vcc, s57, v100
  v_cmpx_ne_u32 vcc, s57, v101
  v_cmpx_ne_u32 vcc, s57, v102
  v_cmpx_ne_u32 vcc, s57, v103
  v_cmpx_ne_u32 vcc, s57, v104
  v_cmpx_ne_u32 vcc, s57, v105
  v_cmpx_ne_u32 vcc, s57, v106
  v_cmpx_ne_u32 vcc, s57, v107
  v_cmpx_ne_u32 vcc, s57, v108
  v_cmpx_ne_u32 vcc, s57, v109
  v_cmpx_ne_u32 vcc, s57, v110
  v_cmpx_ne_u32 vcc, s57, v111
  s_cmp_eq_u64 exec, -1
  s_mov_b64 exec, -1
  s_cbranch_scc1 .Lp2ok
  s_waitcnt lgkmcnt(0)
  s_sub_u32 s59, s59, 1
  s_cbranch_scc0 .Lp2again
  s_branch .Lp2ok
.Lffn:
  s_cmp_ge_u32 s53, 4
  s_addc_u32 s24, s55, s55
  s_lshr_b32 s25, s48, 2
  s_mul_i32 s26, s54, 114688
  s_add_u32 s26, s26, 7589888
  s_mul_i32 s57, s24, 28672
  s_lshl_b32 s58, s48, 12
  s_add_u32 s57, s57, s58
  s_add_u32 s57, s57, s26
  s_mov_b32 m0, s58
  s_cmp_lg_u32 s48, 7
  s_cbranch_scc1 .Lygo
  v_min_u32 v5, 13, v0
  v_lshlrev_b32 v5, 7, v5
  s_movk_i32 s59, 0x7fff
.Lpoll_40:
  buffer_load_dword v6, v5, s[4:7], s0 offen sc1
  s_waitcnt vmcnt(0)
  v_cmp_ne_u32 vcc, s50, v6
  s_cbranch_vccz .Ldone_41
  s_sleep 1
  s_sub_u32 s59, s59, 1
  s_cbranch_scc0 .Lpoll_40
.Ldone_41:
.Lygo:
  s_cmp_eq_u32 s48, 7
  s_waitcnt vmcnt(0) lgkmcnt(0)
  s_barrier
  s_cbranch_scc1 .Lnoy0
  buffer_load_dwordx4 v1, s[4:7], s57 offen sc1 lds
  buffer_load_dwordx4 v1, s[4:7], s57 offen offset:1024 sc1 lds
  buffer_load_dwordx4 v1, s[4:7], s57 offen offset:2048 sc1 lds
  buffer_load_dwordx4 v1, s[4:7], s57 offen offset:3072 sc1 lds
.Lnoy0:
  s_mul_i32 s57, s55, 7
  s_add_u32 s58, s57, s53
  s_lshl3_add_u32 s27, s58, s48
  s_mul_i32 s0, s54, 448
  s_add_u32 s27, s27, s0
  s_lshl_b32 s27, s27, 10
  s_add_u32 s27, s27, 14241792
  s_lshl3_add_u32 s34, s54, s48
  s_lshl_b32 s34, s34, 13
  s_add_u32 s34, s34, 241664
  s_lshl_b32 s28, s58, 7
  s_add_u32 s28, s28, s34
  s_mul_i32 s3, s53, 112
  s_and_b32 s1, s48, 6
  s_add_u32 s3, s3, s1
  s_and_b32 s1, s48, 1
  s_lshl1_add_u32 s3, s3, s1
  s_lshl_b32 s3, s3, 10
  s_add_u32 s3, s3, 12636160
  s_lshl_b32 s57, s57, 13
  s_sub_u32 s58, 57344, s57
  s_lshl1_add_u32 s29, s57, s3
  s_lshl1_add_u32 s30, s58, s3
  s_mul_i32 s3, s55, 224
  s_add_u32 s0, s0, s3
  s_add_u32 s0, s0, s48
  s_lshl_b32 s0, s0, 10
  s_add_u32 s0, s0, 14241792
  s_add_u32 s36, s0, s57
  s_add_u32 s37, s0, s58
  s_lshl_b32 s0, s55, 12
  s_lshr_b32 s57, s57, 6
  s_lshr_b32 s58, s58, 6
  s_add_u32 s38, s0, s57
  s_add_u32 s39, s0, s58
  s_mul_i32 s12, s48, 7168
  s_add_u32 s13, s12, 57344
  s_mul_i32 s14, s49, 7168
  s_xor_b32 s15, s48, 4
  s_lshl3_add_u32 s15, s54, s15
  s_lshl_b32 s15, s15, 13
  s_add_u32 s15, s15, 241664
  s_mul_i32 s35, s1, 7168
  v_mov_b32 v254, v3
  v_add_u32 v252, 57344, v1
  v_lshlrev_b32 v253, 2, v3
  v_add_u32 v253, 118784, v253
  v_add_u32 v5, 7168, v1
  v_add_u32 v6, 14336, v1
  v_add_u32 v7, 21504, v1
  v_min_u32 v4, 6, v0
  v_lshlrev_b32 v4, 7, v4
  s_mov_b64 s[40:41], 15
  s_waitcnt vmcnt(0)
  s_barrier
  s_cmp_eq_u32 s25, 0
  s_cbranch_scc1 .Lx0
  v_min_u32 v8, 27, v0
  v_lshlrev_b32 v8, 2, v8
  s_mul_i32 s0, s49, 112
  s_add_u32 s0, s0, 9216
  buffer_load_dword v9, v8, s[4:7], s0 offen sc1
  s_mov_b32 s1, 1
  s_add_u32 s57, s1, s24
  s_and_b32 s57, s57, 3
  s_lshl2_add_u32 s57, s57, s49
  s_mul_i32 s57, s57, 7168
  s_add_u32 s57, s57, s26
  s_lshl2_add_u32 s1, s1, s49
  s_mul_i32 m0, s1, 7168
  s_nop 0
  buffer_load_dwordx4 v1, s[4:7], s57 offen sc1 lds
  buffer_load_dwordx4 v1, s[4:7], s57 offen offset:1024 sc1 lds
  buffer_load_dwordx4 v1, s[4:7], s57 offen offset:2048 sc1 lds
  buffer_load_dwordx4 v1, s[4:7], s57 offen offset:3072 sc1 lds
  s_add_u32 m0, m0, 4096
  s_add_u32 s57, s57, 4096
  buffer_load_dwordx4 v1, s[4:7], s57 offen sc1 lds
  buffer_load_dwordx4 v1, s[4:7], s57 offen offset:1024 sc1 lds
  buffer_load_dwordx4 v1, s[4:7], s57 offen offset:2048 sc1 lds
  s_add_u32 s57, s21, 16384
  buffer_load_dwordx4 v[200:203], v1, s[4:7], s57 offen offset:2048 sc1
  buffer_load_dwordx4 v[204:207], v1, s[4:7], s57 offen offset:3072 sc1
  s_add_u32 s57, s21, 20480
  buffer_load_dwordx4 v[208:211], v1, s[4:7], s57 offen sc1
  buffer_load_dwordx4 v[212:215], v1, s[4:7], s57 offen offset:1024 sc1
  buffer_load_dwordx4 v[216:219], v1, s[4:7], s57 offen offset:2048 sc1
  buffer_load_dwordx4 v[220:223], v1, s[4:7], s57 offen offset:3072 sc1
  s_add_u32 s57, s21, 24576
  buffer_load_dwordx4 v[224:227], v1, s[4:7], s57 offen sc1
  buffer_load_dwordx4 v[228:231], v1, s[4:7], s57 offen offset:1024 sc1
  buffer_load_dwordx4 v[232:235], v1, s[4:7], s57 offen offset:2048 sc1
  buffer_load_dwordx4 v[236:239], v1, s[4:7], s57 offen offset:3072 sc1
  s_waitcnt vmcnt(17)
  v_cmp_ne_u32 vcc, s50, v9
  s_cbranch_vccz .Ldone_44
  s_movk_i32 s59, 0x7fff
.Lpoll_43:
  buffer_load_dword v9, v8, s[4:7], s0 offen sc1
  s_waitcnt vmcnt(0)
  v_cmp_ne_u32 vcc, s50, v9
  s_cbranch_vccz .Ldone_44
  s_sleep 1
  s_sub_u32 s59, s59, 1
  s_cbranch_scc0 .Lpoll_43
.Ldone_44:
  s_waitcnt vmcnt(10)
  s_barrier
.Lx0:
  ds_read_b128 v[24:27], v1
  ds_read_b128 v[28:31], v1 offset:1024
  ds_read_b128 v[32:35], v1 offset:2048
  ds_read_b128 v[36:39], v1 offset:3072
  s_setprio 1
  s_waitcnt lgkmcnt(3)
  v_mfma_f32_32x32x16_bf16 v[8:23], v[128:131], v[24:27], 0
  ds_read_b128 v[40:43], v1 offset:4096
  s_waitcnt lgkmcnt(3)
  v_mfma_f32_32x32x16_bf16 v[8:23], v[132:135], v[28:31], v[8:23]
  ds_read_b128 v[44:47], v1 offset:5120
  s_waitcnt lgkmcnt(3)
  v_mfma_f32_32x32x16_bf16 v[8:23], v[136:139], v[32:35], v[8:23]
  ds_read_b128 v[24:27], v1 offset:6144
  s_waitcnt lgkmcnt(3)
  v_mfma_f32_32x32x16_bf16 v[8:23], v[140:143], v[36:39], v[8:23]
  ds_read_b128 v[28:31], v1 offset:7168
  s_waitcnt lgkmcnt(3)
  v_mfma_f32_32x32x16_bf16 v[8:23], v[144:147], v[40:43], v[8:23]
  ds_read_b128 v[32:35], v1 offset:8192
  s_waitcnt lgkmcnt(3)
  v_mfma_f32_32x32x16_bf16 v[8:23], v[148:151], v[44:47], v[8:23]
  ds_read_b128 v[36:39], v1 offset:9216
  s_waitcnt lgkmcnt(3)
  v_mfma_f32_32x32x16_bf16 v[8:23], v[152:155], v[24:27], v[8:23]
  ds_read_b128 v[40:43], v1 offset:10240
  s_waitcnt lgkmcnt(3)
  v_mfma_f32_32x32x16_bf16 v[8:23], v[156:159], v[28:31], v[8:23]
  ds_read_b128 v[44:47], v1 offset:11264
  s_waitcnt lgkmcnt(3)
  v_mfma_f32_32x32x16_bf16 v[8:23], v[160:163], v[32:35], v[8:23]
  ds_read_b128 v[24:27], v1 offset:12288
  s_waitcnt lgkmcnt(3)
  v_mfma_f32_32x32x16_bf16 v[8:23], v[164:167], v[36:39], v[8:23]
  ds_read_b128 v[28:31], v1 offset:13312
  s_waitcnt lgkmcnt(3)
  v_mfma_f32_32x32x16_bf16 v[8:23], v[168:171], v[40:43], v[8:23]
  ds_read_b128 v[32:35], v1 offset:14336
  s_waitcnt lgkmcnt(3)
  v_mfma_f32_32x32x16_bf16 v[8:23], v[172:175], v[44:47], v[8:23]
  ds_read_b128 v[36:39], v1 offset:15360
  s_waitcnt lgkmcnt(3)
  v_mfma_f32_32x32x16_bf16 v[8:23], v[176:179], v[24:27], v[8:23]
  ds_read_b128 v[40:43], v1 offset:16384
  s_waitcnt lgkmcnt(3)
  v_mfma_f32_32x32x16_bf16 v[8:23], v[180:183], v[28:31], v[8:23]
  ds_read_b128 v[44:47], v1 offset:17408
  s_waitcnt lgkmcnt(3)
  v_mfma_f32_32x32x16_bf16 v[8:23], v[184:187], v[32:35], v[8:23]
  ds_read_b128 v[24:27], v1 offset:18432
  s_waitcnt lgkmcnt(3)
  v_mfma_f32_32x32x16_bf16 v[8:23], v[188:191], v[36:39], v[8:23]
  ds_read_b128 v[28:31], v1 offset:19456
  s_waitcnt lgkmcnt(3)
  v_mfma_f32_32x32x16_bf16 v[8:23], v[192:195], v[40:43], v[8:23]
  ds_read_b128 v[32:35], v1 offset:20480
  s_waitcnt lgkmcnt(3)
  v_mfma_f32_32x32x16_bf16 v[8:23], v[196:199], v[44:47], v[8:23]
  ds_read_b128 v[36:39], v1 offset:21504
  s_waitcnt vmcnt(0)
  s_waitcnt lgkmcnt(3)
  v_mfma_f32_32x32x16_bf16 v[8:23], v[200:203], v[24:27], v[8:23]
  ds_read_b128 v[40:43], v1 offset:22528
  s_waitcnt lgkmcnt(3)
  v_mfma_f32_32x32x16_bf16 v[8:23], v[204:207], v[28:31], v[8:23]
  ds_read_b128 v[44:47], v1 offset:23552
  s_waitcnt lgkmcnt(3)
  v_mfma_f32_32x32x16_bf16 v[8:23], v[208:211], v[32:35], v[8:23]
  ds_read_b128 v[24:27], v1 offset:24576
  s_waitcnt lgkmcnt(3)
  v_mfma_f32_32x32x16_bf16 v[8:23], v[212:215], v[36:39], v[8:23]
  ds_read_b128 v[28:31], v1 offset:25600
  s_waitcnt lgkmcnt(3)
  v_mfma_f32_32x32x16_bf16 v[8:23], v[216:219], v[40:43], v[8:23]
  ds_read_b128 v[32:35], v1 offset:26624
  s_waitcnt lgkmcnt(3)
  v_mfma_f32_32x32x16_bf16 v[8:23], v[220:223], v[44:47], v[8:23]
  ds_read_b128 v[36:39], v1 offset:27648
  s_waitcnt lgkmcnt(3)
  v_mfma_f32_32x32x16_bf16 v[8:23], v[224:227], v[24:27], v[8:23]
  ds_read_b128 v[40:43], v252 offset:57344
  s_waitcnt lgkmcnt(3)
  v_mfma_f32_32x32x16_bf16 v[8:23], v[228:231], v[28:31], v[8:23]
  ds_read_b128 v[44:47], v253
  s_waitcnt lgkmcnt(3)
  v_mfma_f32_32x32x16_bf16 v[8:23], v[232:235], v[32:35], v[8:23]
  s_setprio 2
  s_waitcnt lgkmcnt(0)
  s_barrier
  v_mfma_f32_32x32x16_bf16 v[8:23], v[236:239], v[36:39], v[8:23]
  v_mfma_f32_32x32x16_bf16 v[8:23], v[240:243], v[40:43], v[8:23]
  s_setprio 0
  s_add_u32 s3, s24, 0
  s_and_b32 s3, s3, 3
  s_mul_i32 s3, s3, 114688
  s_add_u32 s42, s3, s27
  s_add_u32 s1, s25, 2
  s_add_u32 s57, s1, s24
  s_and_b32 s57, s57, 3
  s_lshl2_add_u32 s57, s57, s49
  s_mul_i32 s57, s57, 7168
  s_add_u32 s57, s57, s26
  s_lshl2_add_u32 s1, s1, s49
  s_mul_i32 m0, s1, 7168
  s_nop 0
  buffer_load_dwordx4 v1, s[4:7], s57 offen sc1 lds
  v_mul_f32 v24, v8, v8
  v_mul_f32 v25, v9, v9
  v_mul_f32 v26, v10, v10
  v_mul_f32 v27, v11, v11
  v_mul_f32 v28, v12, v12
  v_mul_f32 v29, v13, v13
  buffer_load_dwordx4 v1, s[4:7], s57 offen offset:1024 sc1 lds
  v_mul_f32 v30, v14, v14
  v_mul_f32 v31, v15, v15
  v_fma_f32 v24, v24, v45, v44
  v_fma_f32 v25, v25, v45, v44
  v_fma_f32 v26, v26, v45, v44
  v_fma_f32 v27, v27, v45, v44
  buffer_load_dwordx4 v1, s[4:7], s57 offen offset:2048 sc1 lds
  v_fma_f32 v28, v28, v45, v44
  v_fma_f32 v29, v29, v45, v44
  v_fma_f32 v30, v30, v45, v44
  v_fma_f32 v31, v31, v45, v44
  v_mul_f32 v24, v24, v8
  v_mul_f32 v25, v25, v9
  buffer_load_dwordx4 v1, s[4:7], s57 offen offset:3072 sc1 lds
  v_mul_f32 v26, v26, v10
  v_mul_f32 v27, v27, v11
  v_mul_f32 v28, v28, v12
  v_mul_f32 v29, v29, v13
  v_mul_f32 v30, v30, v14
  v_mul_f32 v31, v31, v15
  s_add_u32 m0, m0, 4096
  s_add_u32 s57, s57, 4096
  buffer_load_dwordx4 v1, s[4:7], s57 offen sc1 lds
  v_exp_f32 v24, v24
  v_mul_f32 v16, v16, v8
  v_exp_f32 v25, v25
  v_mul_f32 v17, v17, v9
  v_exp_f32 v26, v26
  v_mul_f32 v18, v18, v10
  buffer_load_dwordx4 v1, s[4:7], s57 offen offset:1024 sc1 lds
  v_exp_f32 v27, v27
  v_mul_f32 v19, v19, v11
  v_exp_f32 v28, v28
  v_mul_f32 v20, v20, v12
  v_exp_f32 v29, v29
  v_mul_f32 v21, v21, v13
  buffer_load_dwordx4 v1, s[4:7], s57 offen offset:2048 sc1 lds
  v_exp_f32 v30, v30
  v_mul_f32 v22, v22, v14
  v_exp_f32 v31, v31
  v_mul_f32 v23, v23, v15
  v_fma_f32 v24, v24, v46, v46
  v_fma_f32 v25, v25, v46, v46
  s_add_u32 s58, s29, 0
  buffer_load_dwordx4 v[48:51], v1, s[4:7], s58 offen sc1
  v_fma_f32 v26, v26, v46, v46
  v_fma_f32 v27, v27, v46, v46
  v_fma_f32 v28, v28, v46, v46
  v_fma_f32 v29, v29, v46, v46
  v_fma_f32 v30, v30, v46, v46
  v_fma_f32 v31, v31, v46, v46
  buffer_load_dwordx4 v[52:55], v1, s[4:7], s58 offen offset:2048 sc1
  v_rcp_f32 v24, v24
  v_rcp_f32 v25, v25
  v_rcp_f32 v26, v26
  v_rcp_f32 v27, v27
  v_rcp_f32 v28, v28
  v_rcp_f32 v29, v29
  s_add_u32 s58, s29, 16384
  buffer_load_dwordx4 v[56:59], v1, s[4:7], s58 offen sc1
  v_rcp_f32 v30, v30
  v_rcp_f32 v31, v31
  v_mul_f32 v16, v16, v24
  v_mul_f32 v17, v17, v25
  v_mul_f32 v18, v18, v26
  v_mul_f32 v19, v19, v27
  v_mul_f32 v20, v20, v28
  v_mul_f32 v21, v21, v29
  v_mul_f32 v22, v22, v30
  v_mul_f32 v23, v23, v31
  v_cvt_pk_bf16_f32 v8, v16, v17
  v_cvt_pk_bf16_f32 v9, v18, v19
  v_cvt_pk_bf16_f32 v10, v20, v21
  v_cvt_pk_bf16_f32 v11, v22, v23
  buffer_store_dwordx4 v[8:11], v1, s[4:7], s42 offen sc1
  ds_read_b128 v[24:27], v1 offset:28672
  ds_read_b128 v[28:31], v1 offset:29696
  ds_read_b128 v[32:35], v1 offset:30720
  ds_read_b128 v[36:39], v1 offset:31744
  s_barrier
  s_setprio 1
  s_waitcnt lgkmcnt(3)
  v_mfma_f32_32x32x16_bf16 v[8:23], v[128:131], v[24:27], 0
  ds_read_b128 v[40:43], v1 offset:32768
  s_waitcnt lgkmcnt(3)
  v_mfma_f32_32x32x16_bf16 v[8:23], v[132:135], v[28:31], v[8:23]
  ds_read_b128 v[44:47], v1 offset:33792
  s_waitcnt lgkmcnt(3)
  v_mfma_f32_32x32x16_bf16 v[8:23], v[136:139], v[32:35], v[8:23]
  ds_read_b128 v[24:27], v1 offset:34816
  s_waitcnt lgkmcnt(3)
  v_mfma_f32_32x32x16_bf16 v[8:23], v[140:143], v[36:39], v[8:23]
  ds_read_b128 v[28:31], v1 offset:35840
  s_waitcnt lgkmcnt(3)
  v_mfma_f32_32x32x16_bf16 v[8:23], v[144:147], v[40:43], v[8:23]
  ds_read_b128 v[32:35], v1 offset:36864
  s_waitcnt lgkmcnt(3)
  v_mfma_f32_32x32x16_bf16 v[8:23], v[148:151], v[44:47], v[8:23]
  ds_read_b128 v[36:39], v1 offset:37888
  s_waitcnt lgkmcnt(3)
  v_mfma_f32_32x32x16_bf16 v[8:23], v[152:155], v[24:27], v[8:23]
  ds_read_b128 v[40:43], v1 offset:38912
  s_waitcnt lgkmcnt(3)
  v_mfma_f32_32x32x16_bf16 v[8:23], v[156:159], v[28:31], v[8:23]
  ds_read_b128 v[44:47], v1 offset:39936
  s_waitcnt lgkmcnt(3)
  v_mfma_f32_32x32x16_bf16 v[8:23], v[160:163], v[32:35], v[8:23]
  ds_read_b128 v[24:27], v1 offset:40960
  s_waitcnt lgkmcnt(3)
  v_mfma_f32_32x32x16_bf16 v[8:23], v[164:167], v[36:39], v[8:23]
  ds_read_b128 v[28:31], v1 offset:41984
  s_waitcnt lgkmcnt(3)
  v_mfma_f32_32x32x16_bf16 v[8:23], v[168:171], v[40:43], v[8:23]
  ds_read_b128 v[32:35], v1 offset:43008
  s_waitcnt lgkmcnt(3)
  v_mfma_f32_32x32x16_bf16 v[8:23], v[172:175], v[44:47], v[8:23]
  ds_read_b128 v[36:39], v1 offset:44032
  s_waitcnt lgkmcnt(3)
  v_mfma_f32_32x32x16_bf16 v[8:23], v[176:179], v[24:27], v[8:23]
  ds_read_b128 v[40:43], v1 offset:45056
  s_waitcnt lgkmcnt(3)
  v_mfma_f32_32x32x16_bf16 v[8:23], v[180:183], v[28:31], v[8:23]
  ds_read_b128 v[44:47], v1 offset:46080
  s_waitcnt lgkmcnt(3)
  v_mfma_f32_32x32x16_bf16 v[8:23], v[184:187], v[32:35], v[8:23]
  ds_read_b128 v[24:27], v1 offset:47104
  s_waitcnt lgkmcnt(3)
  v_mfma_f32_32x32x16_bf16 v[8:23], v[188:191], v[36:39], v[8:23]
  ds_read_b128 v[28:31], v1 offset:48128
  s_waitcnt lgkmcnt(3)
  v_mfma_f32_32x32x16_bf16 v[8:23], v[192:195], v[40:43], v[8:23]
  ds_read_b128 v[32:35], v1 offset:49152
  s_waitcnt lgkmcnt(3)
  v_mfma_f32_32x32x16_bf16 v[8:23], v[196:199], v[44:47], v[8:23]
  ds_read_b128 v[36:39], v1 offset:50176
  s_waitcnt vmcnt(0)
  s_add_u32 s0, s24, 0
  s_and_b32 s0, s0, 3
  s_lshl_b32 s0, s0, 11
  s_add_u32 s0, s0, s28
  buffer_store_dword v2, v254, s[4:7], s0 offen sc1
  s_waitcnt lgkmcnt(3)
  v_mfma_f32_32x32x16_bf16 v[8:23], v[200:203], v[24:27], v[8:23]
  ds_read_b128 v[40:43], v1 offset:51200
  s_waitcnt lgkmcnt(3)
  v_mfma_f32_32x32x16_bf16 v[8:23], v[204:207], v[28:31], v[8:23]
  ds_read_b128 v[44:47], v1 offset:52224
  s_waitcnt lgkmcnt(3)
  v_mfma_f32_32x32x16_bf16 v[8:23], v[208:211], v[32:35], v[8:23]
  ds_read_b128 v[24:27], v1 offset:53248
  s_waitcnt lgkmcnt(3)
  v_mfma_f32_32x32x16_bf16 v[8:23], v[212:215], v[36:39], v[8:23]
  ds_read_b128 v[28:31], v1 offset:54272
  s_waitcnt lgkmcnt(3)
  v_mfma_f32_32x32x16_bf16 v[8:23], v[216:219], v[40:43], v[8:23]
  ds_read_b128 v[32:35], v1 offset:55296
  s_add_u32 s58, s29, 16384
  buffer_load_dwordx4 v[60:63], v1, s[4:7], s58 offen offset:2048 sc1
  s_waitcnt lgkmcnt(3)
  v_mfma_f32_32x32x16_bf16 v[8:23], v[220:223], v[44:47], v[8:23]
  ds_read_b128 v[36:39], v1 offset:56320
  s_waitcnt lgkmcnt(3)
  v_mfma_f32_32x32x16_bf16 v[8:23], v[224:227], v[24:27], v[8:23]
  ds_read_b128 v[40:43], v252 offset:58368
  s_add_u32 s58, s29, 32768
  buffer_load_dwordx4 v[64:67], v1, s[4:7], s58 offen sc1
  s_waitcnt lgkmcnt(3)
  v_mfma_f32_32x32x16_bf16 v[8:23], v[228:231], v[28:31], v[8:23]
  ds_read_b128 v[44:47], v253 offset:512
  s_waitcnt lgkmcnt(3)
  v_mfma_f32_32x32x16_bf16 v[8:23], v[232:235], v[32:35], v[8:23]
  buffer_load_dwordx4 v[68:71], v1, s[4:7], s58 offen offset:2048 sc1
  s_waitcnt vmcnt(4)
  s_setprio 2
  s_waitcnt lgkmcnt(0)
  s_barrier
  v_mfma_f32_32x32x16_bf16 v[8:23], v[236:239], v[36:39], v[8:23]
  v_mfma_f32_32x32x16_bf16 v[8:23], v[240:243], v[40:43], v[8:23]
  s_setprio 0
  s_add_u32 s3, s24, 1
  s_and_b32 s3, s3, 3
  s_mul_i32 s3, s3, 114688
  s_add_u32 s42, s3, s27
  s_add_u32 s58, s29, 49152
  buffer_load_dwordx4 v[72:75], v1, s[4:7], s58 offen sc1
  s_nop 4
  v_mul_f32 v24, v8, v8
  v_mul_f32 v25, v9, v9
  v_mul_f32 v26, v10, v10
  v_mul_f32 v27, v11, v11
  v_mul_f32 v28, v12, v12
  v_mul_f32 v29, v13, v13
  buffer_load_dwordx4 v[76:79], v1, s[4:7], s58 offen offset:2048 sc1
  v_mul_f32 v30, v14, v14
  v_mul_f32 v31, v15, v15
  v_fma_f32 v24, v24, v45, v44
  v_fma_f32 v25, v25, v45, v44
  v_fma_f32 v26, v26, v45, v44
  v_fma_f32 v27, v27, v45, v44
  s_add_u32 s58, s29, 65536
  buffer_load_dwordx4 v[80:83], v1, s[4:7], s58 offen sc1
  v_fma_f32 v28, v28, v45, v44
  v_fma_f32 v29, v29, v45, v44
  v_fma_f32 v30, v30, v45, v44
  v_fma_f32 v31, v31, v45, v44
  v_mul_f32 v24, v24, v8
  v_mul_f32 v25, v25, v9
  buffer_load_dwordx4 v[84:87], v1, s[4:7], s58 offen offset:2048 sc1
  v_mul_f32 v26, v26, v10
  v_mul_f32 v27, v27, v11
  v_mul_f32 v28, v28, v12
  v_mul_f32 v29, v29, v13
  v_mul_f32 v30, v30, v14
  v_mul_f32 v31, v31, v15
  s_add_u32 s58, s29, 81920
  buffer_load_dwordx4 v[88:91], v1, s[4:7], s58 offen sc1
  v_exp_f32 v24, v24
  v_mul_f32 v16, v16, v8
  v_exp_f32 v25, v25
  v_mul_f32 v17, v17, v9
  v_exp_f32 v26, v26
  v_mul_f32 v18, v18, v10
  buffer_load_dwordx4 v[92:95], v1, s[4:7], s58 offen offset:2048 sc1
  v_exp_f32 v27, v27
  v_mul_f32 v19, v19, v11
  v_exp_f32 v28, v28
  v_mul_f32 v20, v20, v12
  v_exp_f32 v29, v29
  v_mul_f32 v21, v21, v13
  s_add_u32 s58, s29, 98304
  buffer_load_dwordx4 v[96:99], v1, s[4:7], s58 offen sc1
  v_exp_f32 v30, v30
  v_mul_f32 v22, v22, v14
  v_exp_f32 v31, v31
  v_mul_f32 v23, v23, v15
  v_fma_f32 v24, v24, v46, v46
  v_fma_f32 v25, v25, v46, v46
  buffer_load_dwordx4 v[100:103], v1, s[4:7], s58 offen offset:2048 sc1
  v_fma_f32 v26, v26, v46, v46
  v_fma_f32 v27, v27, v46, v46
  v_fma_f32 v28, v28, v46, v46
  v_fma_f32 v29, v29, v46, v46
  v_fma_f32 v30, v30, v46, v46
  v_fma_f32 v31, v31, v46, v46
  s_add_u32 s58, s30, 65536
  buffer_load_dwordx4 v[104:107], v1, s[4:7], s58 offen sc1
  v_rcp_f32 v24, v24
  v_rcp_f32 v25, v25
  v_rcp_f32 v26, v26
  v_rcp_f32 v27, v27
  v_rcp_f32 v28, v28
  v_rcp_f32 v29, v29
  buffer_load_dwordx4 v[108:111], v1, s[4:7], s58 offen offset:2048 sc1
  v_rcp_f32 v30, v30
  v_rcp_f32 v31, v31
  v_mul_f32 v16, v16, v24
  v_mul_f32 v17, v17, v25
  v_mul_f32 v18, v18, v26
  v_mul_f32 v19, v19, v27
  v_mul_f32 v20, v20, v28
  v_mul_f32 v21, v21, v29
  v_mul_f32 v22, v22, v30
  v_mul_f32 v23, v23, v31
  v_cvt_pk_bf16_f32 v8, v16, v17
  v_cvt_pk_bf16_f32 v9, v18, v19
  v_cvt_pk_bf16_f32 v10, v20, v21
  v_cvt_pk_bf16_f32 v11, v22, v23
  buffer_store_dwordx4 v[8:11], v1, s[4:7], s42 offen sc1
  ds_read_b128 v[24:27], v252
  ds_read_b128 v[28:31], v252 offset:1024
  ds_read_b128 v[32:35], v252 offset:2048
  ds_read_b128 v[36:39], v252 offset:3072
  s_barrier
  s_setprio 1
  s_waitcnt lgkmcnt(3)
  v_mfma_f32_32x32x16_bf16 v[8:23], v[128:131], v[24:27], 0
  ds_read_b128 v[40:43], v252 offset:4096
  s_waitcnt lgkmcnt(3)
  v_mfma_f32_32x32x16_bf16 v[8:23], v[132:135], v[28:31], v[8:23]
  ds_read_b128 v[44:47], v252 offset:5120
  s_waitcnt lgkmcnt(3)
  v_mfma_f32_32x32x16_bf16 v[8:23], v[136:139], v[32:35], v[8:23]
  ds_read_b128 v[24:27], v252 offset:6144
  s_add_u32 s0, s38, 0
  s_add_u32 s3, s38, 2048
  v_add_u32 v255, s0, v4
  v_add_u32 v3, s3, v4
  v_cndmask_b32 v255, v3, v255, s[40:41]
  buffer_load_dword v3, v255, s[4:7], s34 offen sc1
  s_waitcnt lgkmcnt(3)
  v_mfma_f32_32x32x16_bf16 v[8:23], v[140:143], v[36:39], v[8:23]
  ds_read_b128 v[28:31], v252 offset:7168
  s_waitcnt lgkmcnt(3)
  v_mfma_f32_32x32x16_bf16 v[8:23], v[144:147], v[40:43], v[8:23]
  ds_read_b128 v[32:35], v252 offset:8192
  s_waitcnt lgkmcnt(3)
  v_mfma_f32_32x32x16_bf16 v[8:23], v[148:151], v[44:47], v[8:23]
  ds_read_b128 v[36:39], v252 offset:9216
  s_waitcnt lgkmcnt(3)
  v_mfma_f32_32x32x16_bf16 v[8:23], v[152:155], v[24:27], v[8:23]
  ds_read_b128 v[40:43], v252 offset:10240
  s_waitcnt lgkmcnt(3)
  v_mfma_f32_32x32x16_bf16 v[8:23], v[156:159], v[28:31], v[8:23]
  ds_read_b128 v[44:47], v252 offset:11264
  s_waitcnt lgkmcnt(3)
  v_mfma_f32_32x32x16_bf16 v[8:23], v[160:163], v[32:35], v[8:23]
  ds_read_b128 v[24:27], v252 offset:12288
  s_waitcnt lgkmcnt(3)
  v_mfma_f32_32x32x16_bf16 v[8:23], v[164:167], v[36:39], v[8:23]
  ds_read_b128 v[28:31], v252 offset:13312
  s_waitcnt lgkmcnt(3)
  v_mfma_f32_32x32x16_bf16 v[8:23], v[168:171], v[40:43], v[8:23]
  ds_read_b128 v[32:35], v252 offset:14336
  s_waitcnt lgkmcnt(3)
  v_mfma_f32_32x32x16_bf16 v[8:23], v[172:175], v[44:47], v[8:23]
  ds_read_b128 v[36:39], v252 offset:15360
  s_waitcnt lgkmcnt(3)
  v_mfma_f32_32x32x16_bf16 v[8:23], v[176:179], v[24:27], v[8:23]
  ds_read_b128 v[40:43], v252 offset:16384
  s_waitcnt lgkmcnt(3)
  v_mfma_f32_32x32x16_bf16 v[8:23], v[180:183], v[28:31], v[8:23]
  ds_read_b128 v[44:47], v252 offset:17408
  s_waitcnt lgkmcnt(3)
  v_mfma_f32_32x32x16_bf16 v[8:23], v[184:187], v[32:35], v[8:23]
  ds_read_b128 v[24:27], v252 offset:18432
  s_waitcnt lgkmcnt(3)
  v_mfma_f32_32x32x16_bf16 v[8:23], v[188:191], v[36:39], v[8:23]
  ds_read_b128 v[28:31], v252 offset:19456
  s_waitcnt lgkmcnt(3)
  v_mfma_f32_32x32x16_bf16 v[8:23], v[192:195], v[40:43], v[8:23]
  ds_read_b128 v[32:35], v252 offset:20480
  s_waitcnt lgkmcnt(3)
  v_mfma_f32_32x32x16_bf16 v[8:23], v[196:199], v[44:47], v[8:23]
  ds_read_b128 v[36:39], v252 offset:21504
  s_waitcnt vmcnt(1)
  s_add_u32 s0, s24, 1
  s_and_b32 s0, s0, 3
  s_lshl_b32 s0, s0, 11
  s_add_u32 s0, s0, s28
  buffer_store_dword v2, v254, s[4:7], s0 offen sc1
  s_waitcnt lgkmcnt(3)
  v_mfma_f32_32x32x16_bf16 v[8:23], v[200:203], v[24:27], v[8:23]
  ds_read_b128 v[40:43], v252 offset:22528
  s_waitcnt lgkmcnt(3)
  v_mfma_f32_32x32x16_bf16 v[8:23], v[204:207], v[28:31], v[8:23]
  ds_read_b128 v[44:47], v252 offset:23552
  s_waitcnt lgkmcnt(3)
  v_mfma_f32_32x32x16_bf16 v[8:23], v[208:211], v[32:35], v[8:23]
  ds_read_b128 v[24:27], v252 offset:24576
  s_waitcnt lgkmcnt(3)
  v_mfma_f32_32x32x16_bf16 v[8:23], v[212:215], v[36:39], v[8:23]
  ds_read_b128 v[28:31], v252 offset:25600
  s_waitcnt lgkmcnt(3)
  v_mfma_f32_32x32x16_bf16 v[8:23], v[216:219], v[40:43], v[8:23]
  ds_read_b128 v[32:35], v252 offset:26624
  s_add_u32 s58, s30, 81920
  buffer_load_dwordx4 v[112:115], v1, s[4:7], s58 offen sc1
  s_waitcnt lgkmcnt(3)
  v_mfma_f32_32x32x16_bf16 v[8:23], v[220:223], v[44:47], v[8:23]
  ds_read_b128 v[36:39], v252 offset:27648
  s_waitcnt lgkmcnt(3)
  v_mfma_f32_32x32x16_bf16 v[8:23], v[224:227], v[24:27], v[8:23]
  ds_read_b128 v[40:43], v252 offset:59392
  buffer_load_dwordx4 v[116:119], v1, s[4:7], s58 offen offset:2048 sc1
  s_waitcnt lgkmcnt(3)
  v_mfma_f32_32x32x16_bf16 v[8:23], v[228:231], v[28:31], v[8:23]
  ds_read_b128 v[44:47], v253 offset:1024
  s_waitcnt lgkmcnt(3)
  v_mfma_f32_32x32x16_bf16 v[8:23], v[232:235], v[32:35], v[8:23]
  s_add_u32 s58, s30, 98304
  buffer_load_dwordx4 v[120:123], v1, s[4:7], s58 offen sc1
  s_waitcnt vmcnt(5)
  s_setprio 2
  s_waitcnt lgkmcnt(0)
  s_barrier
  v_mfma_f32_32x32x16_bf16 v[8:23], v[236:239], v[36:39], v[8:23]
  v_mfma_f32_32x32x16_bf16 v[8:23], v[240:243], v[40:43], v[8:23]
  s_setprio 0
  s_waitcnt vmcnt(4)
  v_cmp_ne_u32 vcc, s50, v3
  s_cbranch_vccz .Ldone_47
  s_movk_i32 s59, 0x7fff
.Lpoll_46:
  buffer_load_dword v3, v255, s[4:7], s34 offen sc1
  s_waitcnt vmcnt(0)
  v_cmp_ne_u32 vcc, s50, v3
  s_cbranch_vccz .Ldone_47
  s_sleep 1
  s_sub_u32 s59, s59, 1
  s_cbranch_scc0 .Lpoll_46
.Ldone_47:
  s_add_u32 s3, s24, 2
  s_and_b32 s3, s3, 3
  s_mul_i32 s3, s3, 114688
  s_add_u32 s42, s3, s27
  s_mov_b32 m0, s12
  s_add_u32 s57, s36, 0
  buffer_load_dwordx4 v1, s[4:7], s57 offen sc1 lds
  s_nop 0
  v_mul_f32 v24, v8, v8
  v_mul_f32 v25, v9, v9
  v_mul_f32 v26, v10, v10
  v_mul_f32 v27, v11, v11
  v_mul_f32 v28, v12, v12
  v_mul_f32 v29, v13, v13
  buffer_load_dwordx4 v5, s[4:7], s57 offen offset:1024 sc1 lds
  v_mul_f32 v30, v14, v14
  v_mul_f32 v31, v15, v15
  v_fma_f32 v24, v24, v45, v44
  v_fma_f32 v25, v25, v45, v44
  v_fma_f32 v26, v26, v45, v44
  v_fma_f32 v27, v27, v45, v44
  buffer_load_dwordx4 v6, s[4:7], s57 offen offset:2048 sc1 lds
  v_fma_f32 v28, v28, v45, v44
  v_fma_f32 v29, v29, v45, v44
  v_fma_f32 v30, v30, v45, v44
  v_fma_f32 v31, v31, v45, v44
  v_mul_f32 v24, v24, v8
  v_mul_f32 v25, v25, v9
  buffer_load_dwordx4 v7, s[4:7], s57 offen offset:3072 sc1 lds
  v_mul_f32 v26, v26, v10
  v_mul_f32 v27, v27, v11
  v_mul_f32 v28, v28, v12
  v_mul_f32 v29, v29, v13
  v_mul_f32 v30, v30, v14
  v_mul_f32 v31, v31, v15
  s_add_u32 m0, m0, 4096
  s_add_u32 s57, s36, 147456
  buffer_load_dwordx4 v1, s[4:7], s57 offen sc1 lds
  v_exp_f32 v24, v24
  v_mul_f32 v16, v16, v8
  v_exp_f32 v25, v25
  v_mul_f32 v17, v17, v9
  v_exp_f32 v26, v26
  v_mul_f32 v18, v18, v10
  buffer_load_dwordx4 v5, s[4:7], s57 offen offset:1024 sc1 lds
  v_exp_f32 v27, v27
  v_mul_f32 v19, v19, v11
  v_exp_f32 v28, v28
  v_mul_f32 v20, v20, v12
  v_exp_f32 v29, v29
  v_mul_f32 v21, v21, v13
  buffer_load_dwordx4 v6, s[4:7], s57 offen offset:2048 sc1 lds
  v_exp_f32 v30, v30
  v_mul_f32 v22, v22, v14
  v_exp_f32 v31, v31
  v_mul_f32 v23, v23, v15
  v_fma_f32 v24, v24, v46, v46
  v_fma_f32 v25, v25, v46, v46
  s_add_u32 s58, s30, 98304
  buffer_load_dwordx4 v[124:127], v1, s[4:7], s58 offen offset:2048 sc1
  v_fma_f32 v26, v26, v46, v46
  v_fma_f32 v27, v27, v46, v46
  v_fma_f32 v28, v28, v46, v46
  v_fma_f32 v29, v29, v46, v46
  v_fma_f32 v30, v30, v46, v46
  v_fma_f32 v31, v31, v46, v46
  s_add_u32 s58, s30, 0
  buffer_load_dwordx4 v[244:247], v1, s[4:7], s58 offen sc1
  v_rcp_f32 v24, v24
  v_rcp_f32 v25, v25
  v_rcp_f32 v26, v26
  v_rcp_f32 v27, v27
  v_rcp_f32 v28, v28
  v_rcp_f32 v29, v29
  buffer_load_dwordx4 v[248:251], v1, s[4:7], s58 offen offset:2048 sc1
  v_rcp_f32 v30, v30
  v_rcp_f32 v31, v31
  v_mul_f32 v16, v16, v24
  v_mul_f32 v17, v17, v25
  v_mul_f32 v18, v18, v26
  v_mul_f32 v19, v19, v27
  v_mul_f32 v20, v20, v28
  v_mul_f32 v21, v21, v29
  v_mul_f32 v22, v22, v30
  v_mul_f32 v23, v23, v31
  v_cvt_pk_bf16_f32 v8, v16, v17
  v_cvt_pk_bf16_f32 v9, v18, v19
  v_cvt_pk_bf16_f32 v10, v20, v21
  v_cvt_pk_bf16_f32 v11, v22, v23
  buffer_store_dwordx4 v[8:11], v1, s[4:7], s42 offen sc1
  ds_read_b128 v[24:27], v252 offset:28672
  ds_read_b128 v[28:31], v252 offset:29696
  ds_read_b128 v[32:35], v252 offset:30720
  ds_read_b128 v[36:39], v252 offset:31744
  s_barrier
  s_setprio 1
  s_waitcnt lgkmcnt(3)
  v_mfma_f32_32x32x16_bf16 v[8:23], v[128:131], v[24:27], 0
  ds_read_b128 v[40:43], v252 offset:32768
  s_waitcnt lgkmcnt(3)
  v_mfma_f32_32x32x16_bf16 v[8:23], v[132:135], v[28:31], v[8:23]
  ds_read_b128 v[44:47], v252 offset:33792
  s_waitcnt lgkmcnt(3)
  v_mfma_f32_32x32x16_bf16 v[8:23], v[136:139], v[32:35], v[8:23]
  ds_read_b128 v[24:27], v252 offset:34816
  s_add_u32 s0, s38, 2048
  s_add_u32 s3, s39, 0
  v_add_u32 v255, s0, v4
  v_add_u32 v3, s3, v4
  v_cndmask_b32 v255, v3, v255, s[40:41]
  buffer_load_dword v3, v255, s[4:7], s34 offen sc1
  s_waitcnt lgkmcnt(3)
  v_mfma_f32_32x32x16_bf16 v[8:23], v[140:143], v[36:39], v[8:23]
  ds_read_b128 v[28:31], v252 offset:35840
  s_waitcnt lgkmcnt(3)
  v_mfma_f32_32x32x16_bf16 v[8:23], v[144:147], v[40:43], v[8:23]
  ds_read_b128 v[32:35], v252 offset:36864
  s_waitcnt lgkmcnt(3)
  v_mfma_f32_32x32x16_bf16 v[8:23], v[148:151], v[44:47], v[8:23]
  ds_read_b128 v[36:39], v252 offset:37888
  s_waitcnt lgkmcnt(3)
  v_mfma_f32_32x32x16_bf16 v[8:23], v[152:155], v[24:27], v[8:23]
  ds_read_b128 v[40:43], v252 offset:38912
  s_waitcnt lgkmcnt(3)
  v_mfma_f32_32x32x16_bf16 v[8:23], v[156:159], v[28:31], v[8:23]
  ds_read_b128 v[44:47], v252 offset:39936
  s_waitcnt lgkmcnt(3)
  v_mfma_f32_32x32x16_bf16 v[8:23], v[160:163], v[32:35], v[8:23]
  ds_read_b128 v[24:27], v252 offset:40960
  s_waitcnt lgkmcnt(3)
  v_mfma_f32_32x32x16_bf16 v[8:23], v[164:167], v[36:39], v[8:23]
  ds_read_b128 v[28:31], v252 offset:41984
  s_waitcnt lgkmcnt(3)
  v_mfma_f32_32x32x16_bf16 v[8:23], v[168:171], v[40:43], v[8:23]
  ds_read_b128 v[32:35], v252 offset:43008
  s_waitcnt lgkmcnt(3)
  v_mfma_f32_32x32x16_bf16 v[8:23], v[172:175], v[44:47], v[8:23]
  ds_read_b128 v[36:39], v252 offset:44032
  s_waitcnt lgkmcnt(3)
  v_mfma_f32_32x32x16_bf16 v[8:23], v[176:179], v[24:27], v[8:23]
  ds_read_b128 v[40:43], v252 offset:45056
  s_waitcnt lgkmcnt(3)
  v_mfma_f32_32x32x16_bf16 v[8:23], v[180:183], v[28:31], v[8:23]
  ds_read_b128 v[44:47], v252 offset:46080
  s_waitcnt lgkmcnt(3)
  v_mfma_f32_32x32x16_bf16 v[8:23], v[184:187], v[32:35], v[8:23]
  ds_read_b128 v[24:27], v252 offset:47104
  s_waitcnt lgkmcnt(3)
  v_mfma_f32_32x32x16_bf16 v[8:23], v[188:191], v[36:39], v[8:23]
  ds_read_b128 v[28:31], v252 offset:48128
  s_waitcnt lgkmcnt(3)
  v_mfma_f32_32x32x16_bf16 v[8:23], v[192:195], v[40:43], v[8:23]
  ds_read_b128 v[32:35], v252 offset:49152
  s_waitcnt lgkmcnt(3)
  v_mfma_f32_32x32x16_bf16 v[8:23], v[196:199], v[44:47], v[8:23]
  ds_read_b128 v[36:39], v252 offset:50176
  s_waitcnt vmcnt(1)
  s_add_u32 s0, s24, 2
  s_and_b32 s0, s0, 3
  s_lshl_b32 s0, s0, 11
  s_add_u32 s0, s0, s28
  buffer_store_dword v2, v254, s[4:7], s0 offen sc1
  s_waitcnt lgkmcnt(3)
  v_mfma_f32_32x32x16_bf16 v[8:23], v[200:203], v[24:27], v[8:23]
  ds_read_b128 v[40:43], v252 offset:51200
  s_waitcnt lgkmcnt(3)
  v_mfma_f32_32x32x16_bf16 v[8:23], v[204:207], v[28:31], v[8:23]
  ds_read_b128 v[44:47], v252 offset:52224
  s_waitcnt lgkmcnt(3)
  v_mfma_f32_32x32x16_bf16 v[8:23], v[208:211], v[32:35], v[8:23]
  ds_read_b128 v[24:27], v252 offset:53248
  s_waitcnt lgkmcnt(3)
  v_mfma_f32_32x32x16_bf16 v[8:23], v[212:215], v[36:39], v[8:23]
  ds_read_b128 v[28:31], v252 offset:54272
  s_waitcnt lgkmcnt(3)
  v_mfma_f32_32x32x16_bf16 v[8:23], v[216:219], v[40:43], v[8:23]
  ds_read_b128 v[32:35], v252 offset:55296
  s_add_u32 s58, s30, 16384
  buffer_load_dwordx4 v[128:131], v1, s[4:7], s58 offen sc1
  s_waitcnt lgkmcnt(3)
  v_mfma_f32_32x32x16_bf16 v[8:23], v[220:223], v[44:47], v[8:23]
  ds_read_b128 v[36:39], v252 offset:56320
  s_waitcnt lgkmcnt(3)
  v_mfma_f32_32x32x16_bf16 v[8:23], v[224:227], v[24:27], v[8:23]
  ds_read_b128 v[40:43], v252 offset:60416
  buffer_load_dwordx4 v[132:135], v1, s[4:7], s58 offen offset:2048 sc1
  s_waitcnt lgkmcnt(3)
  v_mfma_f32_32x32x16_bf16 v[8:23], v[228:231], v[28:31], v[8:23]
  ds_read_b128 v[44:47], v253 offset:1536
  s_waitcnt lgkmcnt(3)
  v_mfma_f32_32x32x16_bf16 v[8:23], v[232:235], v[32:35], v[8:23]
  s_add_u32 s58, s30, 32768
  buffer_load_dwordx4 v[136:139], v1, s[4:7], s58 offen sc1
  s_waitcnt vmcnt(5)
  s_setprio 2
  s_waitcnt lgkmcnt(0)
  s_barrier
  v_mfma_f32_32x32x16_bf16 v[8:23], v[236:239], v[36:39], v[8:23]
  v_mfma_f32_32x32x16_bf16 v[8:23], v[240:243], v[40:43], v[8:23]
  s_setprio 0
  s_waitcnt vmcnt(4)
  v_cmp_ne_u32 vcc, s50, v3
  s_cbranch_vccz .Ldone_50
  s_movk_i32 s59, 0x7fff
.Lpoll_49:
  buffer_load_dword v3, v255, s[4:7], s34 offen sc1
  s_waitcnt vmcnt(0)
  v_cmp_ne_u32 vcc, s50, v3
  s_cbranch_vccz .Ldone_50
  s_sleep 1
  s_sub_u32 s59, s59, 1
  s_cbranch_scc0 .Lpoll_49
.Ldone_50:
  s_add_u32 s3, s24, 3
  s_and_b32 s3, s3, 3
  s_mul_i32 s3, s3, 114688
  s_add_u32 s42, s3, s27
  s_mov_b32 m0, s13
  s_add_u32 s57, s36, 114688
  buffer_load_dwordx4 v1, s[4:7], s57 offen sc1 lds
  s_nop 0
  v_mul_f32 v24, v8, v8
  v_mul_f32 v25, v9, v9
  v_mul_f32 v26, v10, v10
  v_mul_f32 v27, v11, v11
  v_mul_f32 v28, v12, v12
  v_mul_f32 v29, v13, v13
  buffer_load_dwordx4 v5, s[4:7], s57 offen offset:1024 sc1 lds
  v_mul_f32 v30, v14, v14
  v_mul_f32 v31, v15, v15
  v_fma_f32 v24, v24, v45, v44
  v_fma_f32 v25, v25, v45, v44
  v_fma_f32 v26, v26, v45, v44
  v_fma_f32 v27, v27, v45, v44
  buffer_load_dwordx4 v6, s[4:7], s57 offen offset:2048 sc1 lds
  v_fma_f32 v28, v28, v45, v44
  v_fma_f32 v29, v29, v45, v44
  v_fma_f32 v30, v30, v45, v44
  v_fma_f32 v31, v31, v45, v44
  v_mul_f32 v24, v24, v8
  v_mul_f32 v25, v25, v9
  buffer_load_dwordx4 v7, s[4:7], s57 offen offset:3072 sc1 lds
  v_mul_f32 v26, v26, v10
  v_mul_f32 v27, v27, v11
  v_mul_f32 v28, v28, v12
  v_mul_f32 v29, v29, v13
  v_mul_f32 v30, v30, v14
  v_mul_f32 v31, v31, v15
  s_add_u32 m0, m0, 4096
  s_add_u32 s57, s37, 32768
  buffer_load_dwordx4 v1, s[4:7], s57 offen sc1 lds
  v_exp_f32 v24, v24
  v_mul_f32 v16, v16, v8
  v_exp_f32 v25, v25
  v_mul_f32 v17, v17, v9
  v_exp_f32 v26, v26
  v_mul_f32 v18, v18, v10
  buffer_load_dwordx4 v5, s[4:7], s57 offen offset:1024 sc1 lds
  v_exp_f32 v27, v27
  v_mul_f32 v19, v19, v11
  v_exp_f32 v28, v28
  v_mul_f32 v20, v20, v12
  v_exp_f32 v29, v29
  v_mul_f32 v21, v21, v13
  buffer_load_dwordx4 v6, s[4:7], s57 offen offset:2048 sc1 lds
  v_exp_f32 v30, v30
  v_mul_f32 v22, v22, v14
  v_exp_f32 v31, v31
  v_mul_f32 v23, v23, v15
  v_fma_f32 v24, v24, v46, v46
  v_fma_f32 v25, v25, v46, v46
  s_add_u32 s58, s30, 32768
  buffer_load_dwordx4 v[140:143], v1, s[4:7], s58 offen offset:2048 sc1
  v_fma_f32 v26, v26, v46, v46
  v_fma_f32 v27, v27, v46, v46
  s_cmp_eq_u32 s25, 1
  s_cbranch_scc0 .Lonly_52
  s_barrier
.Lonly_52:
  v_fma_f32 v28, v28, v46, v46
  v_fma_f32 v29, v29, v46, v46
  v_fma_f32 v30, v30, v46, v46
  s_add_u32 s58, s30, 49152
  buffer_load_dwordx4 v[144:147], v1, s[4:7], s58 offen sc1
  v_fma_f32 v31, v31, v46, v46
  v_rcp_f32 v24, v24
  v_rcp_f32 v25, v25
  v_rcp_f32 v26, v26
  v_rcp_f32 v27, v27
  v_rcp_f32 v28, v28
  buffer_load_dwordx4 v[148:151], v1, s[4:7], s58 offen offset:2048 sc1
  v_rcp_f32 v29, v29
  v_rcp_f32 v30, v30
  v_rcp_f32 v31, v31
  v_mul_f32 v16, v16, v24
  v_mul_f32 v17, v17, v25
  v_mul_f32 v18, v18, v26
  v_mul_f32 v19, v19, v27
  v_mul_f32 v20, v20, v28
  v_mul_f32 v21, v21, v29
  v_mul_f32 v22, v22, v30
  v_mul_f32 v23, v23, v31
  v_cvt_pk_bf16_f32 v8, v16, v17
  v_cvt_pk_bf16_f32 v9, v18, v19
  v_cvt_pk_bf16_f32 v10, v20, v21
  v_cvt_pk_bf16_f32 v11, v22, v23
  buffer_store_dwordx4 v[8:11], v1, s[4:7], s42 offen sc1
  s_add_u32 s0, s39, 0
  s_add_u32 s3, s39, 2048
  v_add_u32 v255, s0, v4
  v_add_u32 v3, s3, v4
  v_cndmask_b32 v255, v3, v255, s[40:41]
  buffer_load_dword v3, v255, s[4:7], s34 offen sc1
  s_sub_u32 s1, s12, s35
  v_add_u32 v253, s1, v1
  v_add_u32 v252, 57344, v253
  ds_read_b128 v[24:27], v253
  ds_read_b128 v[28:31], v253 offset:7168
  ds_read_b128 v[32:35], v253 offset:1024
  ds_read_b128 v[36:39], v253 offset:8192
  s_barrier
  s_setprio 1
  s_waitcnt lgkmcnt(3)
  v_mfma_f32_32x32x16_bf16 v[8:23], v[48:51], v[24:27], 0
  ds_read_b128 v[40:43], v253 offset:2048
  s_waitcnt lgkmcnt(3)
  v_mfma_f32_32x32x16_bf16 v[8:23], v[52:55], v[28:31], v[8:23]
  ds_read_b128 v[44:47], v253 offset:9216
  s_waitcnt lgkmcnt(3)
  v_mfma_f32_32x32x16_bf16 v[8:23], v[56:59], v[32:35], v[8:23]
  ds_read_b128 v[24:27], v253 offset:3072
  s_waitcnt lgkmcnt(3)
  v_mfma_f32_32x32x16_bf16 v[8:23], v[60:63], v[36:39], v[8:23]
  ds_read_b128 v[28:31], v253 offset:10240
  s_waitcnt lgkmcnt(3)
  v_mfma_f32_32x32x16_bf16 v[8:23], v[64:67], v[40:43], v[8:23]
  ds_read_b128 v[32:35], v253 offset:4096
  s_waitcnt lgkmcnt(3)
  v_mfma_f32_32x32x16_bf16 v[8:23], v[68:71], v[44:47], v[8:23]
  ds_read_b128 v[36:39], v253 offset:11264
  s_waitcnt lgkmcnt(3)
  v_mfma_f32_32x32x16_bf16 v[8:23], v[72:75], v[24:27], v[8:23]
  ds_read_b128 v[40:43], v253 offset:5120
  s_waitcnt lgkmcnt(3)
  v_mfma_f32_32x32x16_bf16 v[8:23], v[76:79], v[28:31], v[8:23]
  ds_read_b128 v[44:47], v253 offset:12288
  s_waitcnt lgkmcnt(3)
  v_mfma_f32_32x32x16_bf16 v[152:167], v[80:83], v[32:35], 0
  ds_read_b128 v[24:27], v253 offset:6144
  s_waitcnt lgkmcnt(3)
  v_mfma_f32_32x32x16_bf16 v[152:167], v[84:87], v[36:39], v[152:167]
  ds_read_b128 v[28:31], v253 offset:13312
  s_waitcnt lgkmcnt(3)
  v_mfma_f32_32x32x16_bf16 v[152:167], v[88:91], v[40:43], v[152:167]
  s_waitcnt lgkmcnt(2)
  v_mfma_f32_32x32x16_bf16 v[152:167], v[92:95], v[44:47], v[152:167]
  s_waitcnt lgkmcnt(1)
  v_mfma_f32_32x32x16_bf16 v[152:167], v[96:99], v[24:27], v[152:167]
  s_waitcnt vmcnt(5)
  s_barrier
  ds_read_b128 v[32:35], v252
  ds_read_b128 v[36:39], v252 offset:7168
  ds_read_b128 v[40:43], v252 offset:1024
  s_waitcnt lgkmcnt(3)
  v_mfma_f32_32x32x16_bf16 v[152:167], v[100:103], v[28:31], v[152:167]
  ds_read_b128 v[44:47], v252 offset:8192
  s_waitcnt lgkmcnt(3)
  v_mfma_f32_32x32x16_bf16 v[152:167], v[48:51], v[32:35], v[152:167]
  ds_read_b128 v[24:27], v252 offset:2048
  s_waitcnt lgkmcnt(3)
  v_mfma_f32_32x32x16_bf16 v[152:167], v[52:55], v[36:39], v[152:167]
  ds_read_b128 v[28:31], v252 offset:9216
  s_waitcnt lgkmcnt(3)
  v_mfma_f32_32x32x16_bf16 v[152:167], v[56:59], v[40:43], v[152:167]
  ds_read_b128 v[32:35], v252 offset:3072
  s_waitcnt lgkmcnt(3)
  v_mfma_f32_32x32x16_bf16 v[152:167], v[60:63], v[44:47], v[152:167]
  ds_read_b128 v[36:39], v252 offset:10240
  s_waitcnt lgkmcnt(3)
  v_mfma_f32_32x32x16_bf16 v[152:167], v[64:67], v[24:27], v[152:167]
  ds_read_b128 v[40:43], v252 offset:4096
  s_waitcnt lgkmcnt(3)
  v_mfma_f32_32x32x16_bf16 v[152:167], v[68:71], v[28:31], v[152:167]
  ds_read_b128 v[44:47], v252 offset:11264
  s_waitcnt lgkmcnt(3)
  v_mfma_f32_32x32x16_bf16 v[152:167], v[72:75], v[32:35], v[152:167]
  ds_read_b128 v[24:27], v252 offset:5120
  s_waitcnt vmcnt(0)
  s_cmp_eq_u32 s25, 1
  s_cbranch_scc0 .Lgrp_53
  s_add_u32 s0, s39, 2048
  s_add_u32 s3, s38, 0
  v_add_u32 v255, s0, v4
  v_add_u32 v3, s3, v4
  v_cndmask_b32 v255, v3, v255, s[40:41]
  buffer_load_dword v3, v255, s[4:7], s15 offen sc1
.Lgrp_53:
  s_add_u32 s0, s24, 3
  s_and_b32 s0, s0, 3
  s_lshl_b32 s0, s0, 11
  s_add_u32 s0, s0, s28
  buffer_store_dword v2, v254, s[4:7], s0 offen sc1
  s_waitcnt lgkmcnt(3)
  v_mfma_f32_32x32x16_bf16 v[152:167], v[76:79], v[36:39], v[152:167]
  ds_read_b128 v[28:31], v252 offset:12288
  s_waitcnt lgkmcnt(3)
  v_mfma_f32_32x32x16_bf16 v[8:23], v[104:107], v[40:43], v[8:23]
  ds_read_b128 v[32:35], v252 offset:6144
  s_waitcnt lgkmcnt(3)
  v_mfma_f32_32x32x16_bf16 v[8:23], v[108:111], v[44:47], v[8:23]
  ds_read_b128 v[36:39], v252 offset:13312
  s_waitcnt lgkmcnt(3)
  v_mfma_f32_32x32x16_bf16 v[8:23], v[112:115], v[24:27], v[8:23]
  s_waitcnt lgkmcnt(2)
  v_mfma_f32_32x32x16_bf16 v[8:23], v[116:119], v[28:31], v[8:23]
  s_setprio 2
  s_waitcnt lgkmcnt(0)
  s_barrier
  v_mfma_f32_32x32x16_bf16 v[8:23], v[120:123], v[32:35], v[8:23]
  v_mfma_f32_32x32x16_bf16 v[8:23], v[124:127], v[36:39], v[8:23]
  s_setprio 0
  s_cmp_eq_u32 s25, 0
  s_cbranch_scc0 .Ly5
  s_waitcnt vmcnt(1)
  v_cmp_ne_u32 vcc, s50, v3
  s_cbranch_vccz .Ldone_55
  s_movk_i32 s59, 0x7fff
.Lpoll_54:
  buffer_load_dword v3, v255, s[4:7], s34 offen sc1
  s_waitcnt vmcnt(0)
  v_cmp_ne_u32 vcc, s50, v3
  s_cbranch_vccz .Ldone_55
  s_sleep 1
  s_sub_u32 s59, s59, 1
  s_cbranch_scc0 .Lpoll_54
.Ldone_55:
  s_add_u32 s0, s39, 0
  s_add_u32 s3, s39, 2048
  v_add_u32 v255, s0, v4
  v_add_u32 v3, s3, v4
  v_cndmask_b32 v255, v3, v255, s[40:41]
  buffer_load_dword v3, v255, s[4:7], s15 offen sc1
  s_mov_b32 m0, s12
  s_add_u32 s57, s37, 0
  buffer_load_dwordx4 v1, s[4:7], s57 offen sc1 lds
  buffer_load_dwordx4 v5, s[4:7], s57 offen offset:1024 sc1 lds
  buffer_load_dwordx4 v6, s[4:7], s57 offen offset:2048 sc1 lds
  buffer_load_dwordx4 v7, s[4:7], s57 offen offset:3072 sc1 lds
  s_add_u32 m0, m0, 4096
  s_add_u32 s57, s37, 147456
  buffer_load_dwordx4 v1, s[4:7], s57 offen sc1 lds
  buffer_load_dwordx4 v5, s[4:7], s57 offen offset:1024 sc1 lds
  buffer_load_dwordx4 v6, s[4:7], s57 offen offset:2048 sc1 lds
  s_barrier
  s_waitcnt vmcnt(7)
  v_cmp_ne_u32 vcc, s50, v3
  s_cbranch_vccz .Ldone_58
  s_movk_i32 s59, 0x7fff
.Lpoll_57:
  buffer_load_dword v3, v255, s[4:7], s15 offen sc1
  s_waitcnt vmcnt(0)
  v_cmp_ne_u32 vcc, s50, v3
  s_cbranch_vccz .Ldone_58
  s_sleep 1
  s_sub_u32 s59, s59, 1
  s_cbranch_scc0 .Lpoll_57
.Ldone_58:
  s_add_u32 m0, s14, 120832
  s_add_u32 s57, s37, 4096
  buffer_load_dwordx4 v1, s[4:7], s57 offen sc1 lds
  s_mov_b32 s16, s44
  s_and_b32 s17, s45, 0xffff
  s_mov_b32 s18, 1835008
  s_mov_b32 s19, 0x20000
  s_lshr_b32 s1, s48, 1
  s_mul_i32 s58, s1, 17408
  s_and_b32 s1, s48, 1
  buffer_load_dwordx4 v5, s[4:7], s57 offen offset:1024 sc1 lds
  s_lshl_b32 s1, s1, 7
  s_add_u32 s58, s58, s1
  v_and_b32 v200, 31, v0
  v_mul_u32_u24 v200, 272, v200
  v_lshrrev_b32 v204, 5, v0
  v_lshl_add_u32 v200, v204, 4, v200
  v_add_u32 v200, s58, v200
  buffer_load_dwordx4 v6, s[4:7], s57 offen offset:2048 sc1 lds
  v_lshrrev_b32 v204, 3, v0
  v_and_b32 v205, 7, v0
  s_lshl_b32 s1, s48, 10
  s_add_u32 s1, s1, 154624
  v_add_u32 v206, s1, v1
  v_lshlrev_b32 v207, 5, v205
  v_add_u32 v203, 163328, v207
  buffer_load_dwordx4 v7, s[4:7], s57 offen offset:3072 sc1 lds
  ds_read_b128 v[208:211], v206
  ds_read_b128 v[212:215], v203
  ds_read_b128 v[216:219], v203 offset:16
  s_mul_i32 s1, s48, 2176
  v_mul_u32_u24 v201, 272, v204
  v_add3_u32 v201, v201, v207, s1
  v_mul_u32_u24 v203, 896, v204
  s_add_u32 m0, m0, 4096
  s_add_u32 s57, s37, 151552
  buffer_load_dwordx4 v1, s[4:7], s57 offen sc1 lds
  v_lshl_or_b32 v203, v205, 4, v203
  s_lshl1_add_u32 s42, s54, s55
  s_lshl3_add_u32 s42, s42, s48
  s_mul_i32 s42, s42, 7168
  s_lshl_b32 s58, s53, 7
  s_add_u32 s42, s42, s58
  s_waitcnt lgkmcnt(0)
  buffer_load_dwordx4 v5, s[4:7], s57 offen offset:1024 sc1 lds
  v_lshlrev_b32 v220, 16, v208
  v_and_b32 v221, 0xffff0000, v208
  v_lshlrev_b32 v222, 16, v209
  v_and_b32 v223, 0xffff0000, v209
  v_lshlrev_b32 v224, 16, v210
  v_and_b32 v225, 0xffff0000, v210
  v_lshlrev_b32 v226, 16, v211
  buffer_load_dwordx4 v6, s[4:7], s57 offen offset:2048 sc1 lds
  v_and_b32 v227, 0xffff0000, v211
  s_waitcnt vmcnt(7)
  s_branch .Lm5
.Ly5:
  s_waitcnt vmcnt(1)
  v_cmp_ne_u32 vcc, s50, v3
  s_cbranch_vccz .Ldone_61
  s_movk_i32 s59, 0x7fff
.Lpoll_60:
  buffer_load_dword v3, v255, s[4:7], s15 offen sc1
  s_waitcnt vmcnt(0)
  v_cmp_ne_u32 vcc, s50, v3
  s_cbranch_vccz .Ldone_61
  s_sleep 1
  s_sub_u32 s59, s59, 1
  s_cbranch_scc0 .Lpoll_60
.Ldone_61:
  s_add_u32 m0, s14, 57344
  s_add_u32 s57, s37, 110592
  buffer_load_dwordx4 v1, s[4:7], s57 offen sc1 lds
  buffer_load_dwordx4 v5, s[4:7], s57 offen offset:1024 sc1 lds
  buffer_load_dwordx4 v6, s[4:7], s57 offen offset:2048 sc1 lds
  buffer_load_dwordx4 v7, s[4:7], s57 offen offset:3072 sc1 lds
  s_add_u32 m0, m0, 4096
  s_add_u32 s57, s36, 28672
  buffer_load_dwordx4 v1, s[4:7], s57 offen sc1 lds
  buffer_load_dwordx4 v5, s[4:7], s57 offen offset:1024 sc1 lds
  buffer_load_dwordx4 v6, s[4:7], s57 offen offset:2048 sc1 lds
  v_add_u32 v253, 92160, v253
.Lm5:
  s_barrier
  ds_read_b128 v[40:43], v253 offset:4096
  ds_read_b128 v[44:47], v253 offset:11264
  ds_read_b128 v[24:27], v253 offset:5120
  ds_read_b128 v[28:31], v253 offset:12288
  s_setprio 1
  s_waitcnt lgkmcnt(3)
  v_mfma_f32_32x32x16_bf16 v[152:167], v[104:107], v[40:43], v[152:167]
  ds_read_b128 v[32:35], v253 offset:6144
  s_waitcnt lgkmcnt(3)
  v_mfma_f32_32x32x16_bf16 v[152:167], v[108:111], v[44:47], v[152:167]
  ds_read_b128 v[36:39], v253 offset:13312
  s_waitcnt lgkmcnt(3)
  v_mfma_f32_32x32x16_bf16 v[152:167], v[112:115], v[24:27], v[152:167]
  ds_read_b128 v[40:43], v253
  s_waitcnt lgkmcnt(3)
  v_mfma_f32_32x32x16_bf16 v[152:167], v[116:119], v[28:31], v[152:167]
  ds_read_b128 v[44:47], v253 offset:7168
  s_add_u32 s0, s39, 2048
  s_add_u32 s3, s38, 0
  v_add_u32 v255, s0, v4
  v_add_u32 v3, s3, v4
  v_cndmask_b32 v255, v3, v255, s[40:41]
  buffer_load_dword v3, v255, s[4:7], s15 offen sc1
  s_waitcnt lgkmcnt(3)
  v_mfma_f32_32x32x16_bf16 v[152:167], v[120:123], v[32:35], v[152:167]
  ds_read_b128 v[24:27], v253 offset:1024
  s_waitcnt lgkmcnt(3)
  v_mfma_f32_32x32x16_bf16 v[152:167], v[124:127], v[36:39], v[152:167]
  ds_read_b128 v[28:31], v253 offset:8192
  s_waitcnt lgkmcnt(3)
  v_mfma_f32_32x32x16_bf16 v[8:23], v[244:247], v[40:43], v[8:23]
  ds_read_b128 v[32:35], v253 offset:2048
  s_waitcnt lgkmcnt(3)
  v_mfma_f32_32x32x16_bf16 v[8:23], v[248:251], v[44:47], v[8:23]
  ds_read_b128 v[36:39], v253 offset:9216
  s_waitcnt lgkmcnt(3)
  v_mfma_f32_32x32x16_bf16 v[8:23], v[128:131], v[24:27], v[8:23]
  ds_read_b128 v[40:43], v253 offset:3072
  s_waitcnt lgkmcnt(3)
  v_mfma_f32_32x32x16_bf16 v[8:23], v[132:135], v[28:31], v[8:23]
  ds_read_b128 v[44:47], v253 offset:10240
  s_waitcnt lgkmcnt(3)
  v_mfma_f32_32x32x16_bf16 v[8:23], v[136:139], v[32:35], v[8:23]
  s_waitcnt lgkmcnt(2)
  v_mfma_f32_32x32x16_bf16 v[8:23], v[140:143], v[36:39], v[8:23]
  s_waitcnt vmcnt(1)
  s_setprio 2
  s_waitcnt lgkmcnt(0)
  s_barrier
  v_mfma_f32_32x32x16_bf16 v[8:23], v[144:147], v[40:43], v[8:23]
  v_mfma_f32_32x32x16_bf16 v[8:23], v[148:151], v[44:47], v[8:23]
  s_setprio 0
  s_cmp_eq_u32 s25, 0
  s_cbranch_scc0 .Ly6
  s_waitcnt vmcnt(0)
  v_cmp_ne_u32 vcc, s50, v3
  s_cbranch_vccz .Ldone_64
  s_movk_i32 s59, 0x7fff
.Lpoll_63:
  buffer_load_dword v3, v255, s[4:7], s15 offen sc1
  s_waitcnt vmcnt(0)
  v_cmp_ne_u32 vcc, s50, v3
  s_cbranch_vccz .Ldone_64
  s_sleep 1
  s_sub_u32 s59, s59, 1
  s_cbranch_scc0 .Lpoll_63
.Ldone_64:
  s_add_u32 m0, s14, 86016
  s_add_u32 s57, s37, 118784
  buffer_load_dwordx4 v1, s[4:7], s57 offen sc1 lds
  buffer_load_dwordx4 v5, s[4:7], s57 offen offset:1024 sc1 lds
  buffer_load_dwordx4 v6, s[4:7], s57 offen offset:2048 sc1 lds
  buffer_load_dwordx4 v7, s[4:7], s57 offen offset:3072 sc1 lds
  s_add_u32 m0, m0, 4096
  s_add_u32 s57, s36, 36864
  buffer_load_dwordx4 v1, s[4:7], s57 offen sc1 lds
  buffer_load_dwordx4 v5, s[4:7], s57 offen offset:1024 sc1 lds
  buffer_load_dwordx4 v6, s[4:7], s57 offen offset:2048 sc1 lds
  s_branch .Lm6
.Ly6:
  s_mov_b32 s16, s44
  s_and_b32 s17, s45, 0xffff
  s_mov_b32 s18, 1835008
  s_mov_b32 s19, 0x20000
  s_lshr_b32 s1, s48, 1
  s_mul_i32 s58, s1, 17408
  s_and_b32 s1, s48, 1
  s_lshl_b32 s1, s1, 7
  s_add_u32 s58, s58, s1
  v_and_b32 v200, 31, v0
  v_mul_u32_u24 v200, 272, v200
  v_lshrrev_b32 v204, 5, v0
  v_lshl_add_u32 v200, v204, 4, v200
  v_add_u32 v200, s58, v200
  v_lshrrev_b32 v204, 3, v0
  v_and_b32 v205, 7, v0
  s_lshl_b32 s1, s48, 10
  s_add_u32 s1, s1, 154624
  v_add_u32 v206, s1, v1
  v_lshlrev_b32 v207, 5, v205
  v_add_u32 v203, 163328, v207
  ds_read_b128 v[208:211], v206
  ds_read_b128 v[212:215], v203
  ds_read_b128 v[216:219], v203 offset:16
  s_mul_i32 s1, s48, 2176
  v_mul_u32_u24 v201, 272, v204
  v_add3_u32 v201, v201, v207, s1
  v_mul_u32_u24 v203, 896, v204
  v_lshl_or_b32 v203, v205, 4, v203
  s_lshl1_add_u32 s42, s54, s55
  s_lshl3_add_u32 s42, s42, s48
  s_mul_i32 s42, s42, 7168
  s_lshl_b32 s58, s53, 7
  s_add_u32 s42, s42, s58
  s_waitcnt lgkmcnt(0)
  v_lshlrev_b32 v220, 16, v208
  v_and_b32 v221, 0xffff0000, v208
  v_lshlrev_b32 v222, 16, v209
  v_and_b32 v223, 0xffff0000, v209
  v_lshlrev_b32 v224, 16, v210
  v_and_b32 v225, 0xffff0000, v210
  v_lshlrev_b32 v226, 16, v211
  v_and_b32 v227, 0xffff0000, v211
.Lm6:
  s_barrier
  ds_read_b128 v[24:27], v252 offset:4096
  ds_read_b128 v[28:31], v252 offset:11264
  ds_read_b128 v[32:35], v252 offset:5120
  ds_read_b128 v[36:39], v252 offset:12288
  s_setprio 1
  s_waitcnt lgkmcnt(3)
  v_mfma_f32_32x32x16_bf16 v[8:23], v[80:83], v[24:27], v[8:23]
  ds_read_b128 v[40:43], v252 offset:6144
  s_waitcnt lgkmcnt(3)
  v_mfma_f32_32x32x16_bf16 v[8:23], v[84:87], v[28:31], v[8:23]
  ds_read_b128 v[44:47], v252 offset:13312
  s_waitcnt lgkmcnt(3)
  v_mfma_f32_32x32x16_bf16 v[8:23], v[88:91], v[32:35], v[8:23]
  ds_read_b128 v[24:27], v252
  s_waitcnt lgkmcnt(3)
  v_mfma_f32_32x32x16_bf16 v[8:23], v[92:95], v[36:39], v[8:23]
  ds_read_b128 v[28:31], v252 offset:7168
  s_waitcnt lgkmcnt(3)
  v_mfma_f32_32x32x16_bf16 v[8:23], v[96:99], v[40:43], v[8:23]
  ds_read_b128 v[32:35], v252 offset:1024
  s_waitcnt lgkmcnt(3)
  v_mfma_f32_32x32x16_bf16 v[8:23], v[100:103], v[44:47], v[8:23]
  ds_read_b128 v[36:39], v252 offset:8192
  s_waitcnt lgkmcnt(3)
  v_mfma_f32_32x32x16_bf16 v[152:167], v[244:247], v[24:27], v[152:167]
  ds_read_b128 v[40:43], v252 offset:2048
  s_waitcnt lgkmcnt(3)
  v_mfma_f32_32x32x16_bf16 v[152:167], v[248:251], v[28:31], v[152:167]
  ds_read_b128 v[44:47], v252 offset:9216
  s_waitcnt lgkmcnt(3)
  v_mfma_f32_32x32x16_bf16 v[152:167], v[128:131], v[32:35], v[152:167]
  ds_read_b128 v[24:27], v252 offset:3072
  s_waitcnt lgkmcnt(3)
  v_mfma_f32_32x32x16_bf16 v[152:167], v[132:135], v[36:39], v[152:167]
  ds_read_b128 v[28:31], v252 offset:10240
  s_waitcnt lgkmcnt(3)
  v_mfma_f32_32x32x16_bf16 v[152:167], v[136:139], v[40:43], v[152:167]
  s_waitcnt lgkmcnt(2)
  v_mfma_f32_32x32x16_bf16 v[152:167], v[140:143], v[44:47], v[152:167]
  s_waitcnt vmcnt(0)
  s_waitcnt lgkmcnt(0)
  s_cmp_eq_u32 s25, 0
  s_cbranch_scc0 .Lonly_66
  s_barrier
.Lonly_66:
  v_mfma_f32_32x32x16_bf16 v[152:167], v[144:147], v[24:27], v[152:167]
  ds_write_b128 v200, v[8:11]
  ds_write_b128 v200, v[12:15] offset:32
  v_mfma_f32_32x32x16_bf16 v[152:167], v[148:151], v[28:31], v[152:167]
  ds_write_b128 v200, v[16:19] offset:64
  ds_write_b128 v200, v[20:23] offset:96
  s_setprio 0
  s_cmp_eq_u32 s25, 0
  s_cbranch_scc0 .Ly7
  s_sleep 1
  s_nop 5
  ds_write_b128 v200, v[152:155] offset:8704
  s_sleep 1
  ds_write_b128 v200, v[156:159] offset:8736
  s_sleep 1
  ds_write_b128 v200, v[160:163] offset:8768
  s_sleep 1
  ds_write_b128 v200, v[164:167] offset:8800
  s_waitcnt lgkmcnt(0)
  s_barrier
  ds_read_b128 v[64:67], v201
  ds_read_b128 v[68:71], v201 offset:16
  ds_read_b128 v[72:75], v201 offset:17408
  ds_read_b128 v[76:79], v201 offset:17424
  ds_read_b128 v[80:83], v201 offset:34816
  ds_read_b128 v[84:87], v201 offset:34832
  ds_read_b128 v[88:91], v201 offset:52224
  ds_read_b128 v[92:95], v201 offset:52240
  s_barrier
  s_waitcnt lgkmcnt(4)
  v_pk_add_f32 v[64:65], v[64:65], v[72:73]
  v_pk_add_f32 v[66:67], v[66:67], v[74:75]
  v_pk_add_f32 v[68:69], v[68:69], v[76:77]
  v_pk_add_f32 v[70:71], v[70:71], v[78:79]
  s_branch .Lfin
.Ly7:
  s_nop 6
  ds_write_b128 v200, v[152:155] offset:8704
  ds_write_b128 v200, v[156:159] offset:8736
  ds_write_b128 v200, v[160:163] offset:8768
  ds_write_b128 v200, v[164:167] offset:8800
  s_waitcnt lgkmcnt(4)
  s_barrier
  ds_read_b128 v[64:67], v201
  ds_read_b128 v[68:71], v201 offset:16
  ds_read_b128 v[72:75], v201 offset:17408
  ds_read_b128 v[76:79], v201 offset:17424
  s_waitcnt lgkmcnt(4)
  s_barrier
  ds_read_b128 v[80:83], v201 offset:34816
  ds_read_b128 v[84:87], v201 offset:34832
  ds_read_b128 v[88:91], v201 offset:52224
  ds_read_b128 v[92:95], v201 offset:52240
  s_waitcnt lgkmcnt(4)
  v_pk_add_f32 v[64:65], v[64:65], v[72:73]
  v_pk_add_f32 v[66:67], v[66:67], v[74:75]
  v_pk_add_f32 v[68:69], v[68:69], v[76:77]
  v_pk_add_f32 v[70:71], v[70:71], v[78:79]
.Lfin:
  s_waitcnt lgkmcnt(2)
  v_pk_add_f32 v[64:65], v[64:65], v[80:81]
  v_pk_add_f32 v[66:67], v[66:67], v[82:83]
  v_pk_add_f32 v[68:69], v[68:69], v[84:85]
  v_pk_add_f32 v[70:71], v[70:71], v[86:87]
  s_waitcnt lgkmcnt(0)
  v_pk_add_f32 v[64:65], v[64:65], v[88:89]
  v_pk_add_f32 v[66:67], v[66:67], v[90:91]
  v_pk_add_f32 v[68:69], v[68:69], v[92:93]
  v_pk_add_f32 v[70:71], v[70:71], v[94:95]
  v_pk_fma_f32 v[64:65], v[64:65], v[212:213], v[220:221]
  v_pk_fma_f32 v[66:67], v[66:67], v[214:215], v[222:223]
  v_pk_fma_f32 v[68:69], v[68:69], v[216:217], v[224:225]
  v_pk_fma_f32 v[70:71], v[70:71], v[218:219], v[226:227]
  v_cvt_pk_bf16_f32 v72, v64, v65
  v_cvt_pk_bf16_f32 v73, v66, v67
  v_cvt_pk_bf16_f32 v74, v68, v69
  v_cvt_pk_bf16_f32 v75, v70, v71
  buffer_store_dwordx4 v[72:75], v203, s[16:19], s42 offen
.Lexit:
.Lend:
)ASM" ::"{s[0:1]}"(__builtin_amdgcn_kernarg_segment_ptr()), "{s2}"(__builtin_amdgcn_workgroup_id_x()), "{v0}"(__builtin_amdgcn_workitem_id_x()),
               "{s3}"((unsigned)(size_t)(__attribute__((address_space(3))) char*)lds)
               : "memory", "vcc", "scc", "v1", "v2", "v3", "v4", "v5", "v6", "v7", "v8", "v9", "v10", "v11", "v12", "v13", "v14", "v15", "v16", "v17", "v18", "v19", "v20", "v21", "v22", "v23", "v24", "v25", "v26", "v27", "v28", "v29", "v30", "v31", "v32", "v33", "v34", "v35", "v36", "v37", "v38", "v39", "v40", "v41", "v42", "v43", "v44", "v45", "v46", "v47", "v48", "v49", "v50", "v51", "v52", "v53", "v54", "v55", "v56", "v57", "v58", "v59", "v60", "v61", "v62", "v63", "v64", "v65", "v66", "v67", "v68", "v69", "v70", "v71", "v72", "v73", "v74", "v75", "v76", "v77", "v78", "v79", "v80", "v81", "v82", "v83", "v84", "v85", "v86", "v87", "v88", "v89", "v90", "v91", "v92", "v93", "v94", "v95", "v96", "v97", "v98", "v99", "v100", "v101", "v102", "v103", "v104", "v105", "v106", "v107", "v108", "v109", "v110", "v111", "v112", "v113", "v114", "v115", "v116", "v117", "v118", "v119", "v120", "v121", "v122", "v123", "v124", "v125", "v126", "v127", "v128", "v129", "v130", "v131", "v132", "v133", "v134", "v135", "v136", "v137", "v138", "v139", "v140", "v141", "v142", "v143", "v144", "v145", "v146", "v147", "v148", "v149", "v150", "v151", "v152", "v153", "v154", "v155", "v156", "v157", "v158", "v159", "v160", "v161", "v162", "v163", "v164", "v165", "v166", "v167", "v168", "v169", "v170", "v171", "v172", "v173", "v174", "v175", "v176", "v177", "v178", "v179", "v180", "v181", "v182", "v183", "v184", "v185", "v186", "v187", "v188", "v189", "v190", "v191", "v192", "v193", "v194", "v195", "v196", "v197", "v198", "v199", "v200", "v201", "v202", "v203", "v204", "v205", "v206", "v207", "v208", "v209", "v210", "v211", "v212", "v213", "v214", "v215", "v216", "v217", "v218", "v219", "v220", "v221", "v222", "v223", "v224", "v225", "v226", "v227", "v228", "v229", "v230", "v231", "v232", "v233", "v234", "v235", "v236", "v237", "v238", "v239", "v240", "v241", "v242", "v243", "v244", "v245", "v246", "v247", "v248", "v249", "v250", "v251", "v252", "v253", "v254", "v255", "s4", "s5", "s6", "s7", "s8", "s9", "s10", "s11", "s12", "s13", "s14", "s15", "s16", "s17", "s18", "s19", "s20", "s21", "s22", "s23", "s24", "s25", "s26", "s27", "s28", "s29", "s30", "s31", "s33", "s34", "s35", "s36", "s37", "s38", "s39", "s40", "s41", "s42", "s43", "s44", "s45", "s46", "s47", "s48", "s49", "s50", "s51", "s52", "s53", "s54", "s55", "s56", "s57", "s58", "s59", "s60", "s61", "s62", "s63", "s64", "s65", "s66", "s67", "s68", "s69", "s70", "s71", "s72", "s73", "s74", "s75", "s76", "s77", "s78", "s79", "s80", "s81", "s82", "s83", "s84", "s85", "s86", "s87", "s88", "s89", "s90", "s91", "s92", "s93", "s94", "s95", "s96", "s97", "s98", "s99");
}

extern "C" void kernel_launch(void* const* d_in, const int* in_sizes, int n_in, void* d_out, int out_size, void* d_ws, size_t ws_size,
                              hipStream_t stream) {
  fused_block<<<256, 512, 0, stream>>>(d_in[0], d_in[1], d_in[2], d_in[3], d_in[4], d_in[5], d_in[6], d_in[7], d_in[8], d_in[9], d_out, d_ws);
}
```
